```python
import torch

def _softmax(x, axis=-1):
    return torch.softmax(x, dim=axis)
def _gelu(x):
    return torch.nn.functional.gelu(x, approximate="tanh")
def _silu(x):
    return torch.nn.functional.silu(x)
def _sigmoid(x):
    return torch.sigmoid(x)
def _layernorm(x, axis=-1, eps=1e-5):
    m = x.mean(dim=axis, keepdim=True)
    v = x.var(dim=axis, keepdim=True, unbiased=False)
    return (x - m) / torch.sqrt(v + eps)
def _scatter_add(tgt, idx, src, axis=0):
    return tgt.clone().index_add_(axis, idx, src)
def _segment_sum(src, seg, n):
    out = torch.zeros((n,) + src.shape[1:], dtype=src.dtype)
    return out.index_add_(0, seg, src)
def _pad(x, pw):
    flat = tuple(v for pair in reversed(pw) for v in pair)
    return torch.nn.functional.pad(x, flat)

kernel_name = 'sequential_preLN__sliding_local_dilated__mixer_channel_mlp__prompt'

def setup_inputs():

    _w = (lambda a: (lambda t: t.to(torch.bfloat16) if t.is_floating_point() else t)(torch.as_tensor(a)))
    def _A_setup():
        B, S, D = 1, 2048, 576
        return {
            "x": _w((lambda *s: torch.randn(*s, dtype=torch.bfloat16) * 0.05)(B, S, D)),
            "wqkv": _w((lambda *s: torch.randn(*s, dtype=torch.bfloat16) * 0.05)(D, 3 * D)),
            "wo": _w((lambda *s: torch.randn(*s, dtype=torch.bfloat16) * 0.05)(D, D)),
        }
    _A_in = _A_setup()

    def _B_setup():
        B, S, D, F = 1, 2048, 576, 4608
        return {
            "x": _w((lambda *s: torch.randn(*s, dtype=torch.bfloat16) * 0.05)(B, S, D)),
            "w1": _w((lambda *s: torch.randn(*s, dtype=torch.bfloat16) * 3.2)(D, F) * (D ** -0.5)),
            "b1": _w((lambda *s: torch.randn(*s, dtype=torch.bfloat16) * 1.6)(F)),
            "w2": _w((lambda *s: torch.randn(*s, dtype=torch.bfloat16) * 0.2)(F, D) * (F ** -0.5)),
            "b2": _w((lambda *s: torch.randn(*s, dtype=torch.bfloat16) * 0.4)(D)),
        }
    _B_in = _B_setup()

    return {
        "A_x": _A_in["x"],
        "A_wqkv": _A_in["wqkv"],
        "A_wo": _A_in["wo"],
        "B_x": _B_in["x"],
        "B_w1": _B_in["w1"],
        "B_b1": _B_in["b1"],
        "B_w2": _B_in["w2"],
        "B_b2": _B_in["b2"],
    }

def reference(A_x, A_wqkv, A_wo, B_x, B_w1, B_b1, B_w2, B_b2):
    def _A_ref(x, wqkv, wo):
        B, S, D = x.shape
        H = 32; Dh = D // H
        W, dil = 8, 2
        qkv = (lambda x, *d: x.permute(*d))((x @ wqkv).reshape(B, S, 3, H, Dh), 2, 0, 3, 1, 4)
        q, k, v = qkv[0], qkv[1], qkv[2]
        scale = Dh ** -0.5
        pos = torch.arange(S)
        off = torch.arange(W) * dil
        idx = pos[:, None] - off[None, :]
        mask = idx >= 0
        idx_c = torch.where(mask, idx, 0).reshape(-1)
        kg = k[:, :, idx_c, :].reshape(B, H, S, W, Dh)
        vg = v[:, :, idx_c, :].reshape(B, H, S, W, Dh)
        logits = torch.einsum("bhsd,bhswd->bhsw", q, kg) * scale
        logits = torch.where(mask[None, None, :, :], logits, -1e9)
        attn = _softmax(logits, axis=-1)
        o = torch.einsum("bhsw,bhswd->bhsd", attn, vg)
        return (lambda x, *d: x.permute(*d))(o, 0, 2, 1, 3).reshape(B, S, D) @ wo

    def _B_ref(x, w1, b1, w2, b2):
        y = _layernorm(x, axis=-1)
        h = _gelu(torch.einsum("bsd,df->bsf", y, w1) + b1)
        return torch.einsum("bsf,fd->bsd", h, w2) + b2

    x = A_x
    h = x + _A_ref(_layernorm(x, axis=-1), A_wqkv, A_wo)
    h2 = h + _B_ref(_layernorm(h, axis=-1), B_w1, B_b1, B_w2, B_b2)
    return h2
```

```cpp
#include <hip/hip_runtime.h>
#include <hip/hip_bf16.h>
#include <stdint.h>
#include <type_traits>


typedef short s16x4 __attribute__((ext_vector_type(4)));
typedef short s16x8 __attribute__((ext_vector_type(8)));
typedef __bf16 bf2_t __attribute__((ext_vector_type(2)));
typedef float f32x4 __attribute__((ext_vector_type(4)));
typedef float f32x16 __attribute__((ext_vector_type(16)));
typedef unsigned u32x4 __attribute__((ext_vector_type(4)));
typedef unsigned u32x2 __attribute__((ext_vector_type(2)));
typedef unsigned short u16;
typedef __attribute__((address_space(3))) const char* lds_cptr;
typedef __attribute__((address_space(3))) char* lds_ptr;
typedef __attribute__((address_space(3))) s16x4* lds_s16x4_ptr;

#define DEV __device__ __forceinline__
#define MFMA32(a, b, c) __builtin_amdgcn_mfma_f32_32x32x16_bf16(a, b, c, 0, 0, 0)
#define MFMA16(a, b, c) __builtin_amdgcn_mfma_f32_16x16x32_bf16(a, b, c, 0, 0, 0)

constexpr int S_ = 2048, D_ = 576, F_ = 4608;
constexpr float EPS = 1e-5f;

DEV s16x4 vtr(lds_cptr p) { return __builtin_amdgcn_ds_read_tr16_b64_v4i16((lds_s16x4_ptr)p); }
DEV s16x8 cat8(s16x4 lo, s16x4 hi) { return __builtin_shufflevector(lo, hi, 0, 1, 2, 3, 4, 5, 6, 7); }
DEV unsigned pack_bf16(float a, float b) {
  bf2_t v; v[0] = (__bf16)a; v[1] = (__bf16)b; return __builtin_bit_cast(unsigned, v);
}
DEV float bf_lo(unsigned u) { return __builtin_bit_cast(float, u << 16); }
DEV float bf_hi(unsigned u) { return __builtin_bit_cast(float, u & 0xffff0000u); }
DEV float dot2bf(unsigned a, unsigned b, float c) {
  return __builtin_amdgcn_fdot2_f32_bf16(__builtin_bit_cast(bf2_t, a), __builtin_bit_cast(bf2_t, b), c, false);
}
DEV void glds16(unsigned voff, const void* sbase_, unsigned ldsaddr_) {
  unsigned sv;
  const uint64_t pv = (uint64_t)sbase_;
  const uint64_t sbase = ((uint64_t)(unsigned)__builtin_amdgcn_readfirstlane((int)(pv >> 32)) << 32) |
                         (uint64_t)(unsigned)__builtin_amdgcn_readfirstlane((int)(unsigned)pv);
  const unsigned ldsaddr = (unsigned)__builtin_amdgcn_readfirstlane((int)ldsaddr_);
  asm volatile("s_mov_b32 %0, m0\n\ts_mov_b32 m0, %3\n\ts_nop 0\n\tglobal_load_lds_dwordx4 %1, %2\n\ts_mov_b32 m0, %0"
               : "=&s"(sv) : "v"(voff), "s"(sbase), "s"(ldsaddr) : "memory");
}
#define WAIT_BAR(N) asm volatile("s_waitcnt vmcnt(" #N ") lgkmcnt(0)\n\ts_barrier" ::: "memory")

template <int N> DEV void wait_bar() { asm volatile("s_waitcnt vmcnt(%0) lgkmcnt(0)\n\ts_barrier" ::"i"(N) : "memory"); }
template <int B, int E, class F> DEV void static_for(F&& f) {
  if constexpr (B < E) { f(std::integral_constant<int, B>{}); static_for<B + 1, E>(f); }
}
#define AINL __attribute__((always_inline))

DEV float gelu_tanh(float x) {
  const float c1 = -2.0f * 1.4426950408889634f * 0.7978845608028654f;
  const float c3 = c1 * 0.044715f;
  float x2 = x * x;
  float t = x * __builtin_fmaf(x2, c3, c1);
  float e = __builtin_amdgcn_exp2f(t);
  return x * __builtin_amdgcn_rcpf(1.0f + e);
}

constexpr int K1_ROW = 240;
constexpr int K1_W = 576 * K1_ROW;
constexpr int K1_LDS = K1_W + 1024;
constexpr int K1_QS = 232;

__global__ __launch_bounds__(256, 1) void k1_qkv_attn(const u16* __restrict__ x, const u16* __restrict__ wqkv,
                                                      u16* __restrict__ o) {
  extern __shared__ __attribute__((aligned(16))) char smem[];
  const int tid = threadIdx.x, lane = tid & 63;
  const int wid = __builtin_amdgcn_readfirstlane(tid >> 6);
  const int b = blockIdx.x, xcd = b & 7, bi = b >> 3;
  const int hp = 2 * xcd + (bi & 1), pnl = bi >> 1;
  const int par = hp & 1;
  const int i16 = lane & 15, g = lane >> 4, q4 = i16 >> 2, p4 = i16 & 3;
  const unsigned lds0 = (unsigned)(uintptr_t)smem;
  const int T0 = pnl * 128 + wid * 32;

  s16x8 xb[3][18];
#pragma unroll
  for (int ct = 0; ct < 3; ++ct) {
    int tok = T0 - 16 + ct * 16 + i16;
    tok = tok < 0 ? 0 : tok;
    const u16* xr = x + (size_t)tok * D_ + 8 * g;
#pragma unroll
    for (int ks = 0; ks < 18; ++ks) xb[ct][ks] = *(const s16x8*)(xr + 32 * ks);
  }
  {
    const int segbase = (72 * hp) & ~15;
#pragma unroll
    for (int st = 0; st < 3; ++st) {
#pragma unroll
      for (int j = 0; j < 12; ++j) {
        int n = j * 4 + wid;
        n = n > 44 ? 44 : n;
        const int idx = (st * 45 + n) * 64 + lane;
        const int row = idx / 15, ch = idx - row * 15;
        const int seg = ch / 5, cc = ch - seg * 5;
        const unsigned voff = (unsigned)(row * 3456 + seg * 1152 + segbase + cc * 16);
        glds16(voff, wqkv, lds0 + (unsigned)((st * 45 + n) * 1024));
      }
    }
  }
  float ssum[3], ssq[3];
#pragma unroll
  for (int ct = 0; ct < 3; ++ct) {
    float s = 0.f, s2 = 0.f;
#pragma unroll
    for (int ks = 0; ks < 18; ++ks) {
      u32x4 v = __builtin_bit_cast(u32x4, xb[ct][ks]);
#pragma unroll
      for (int e = 0; e < 4; ++e) { s = dot2bf(v[e], 0x3f803f80u, s); s2 = dot2bf(v[e], v[e], s2); }
    }
    s += __shfl_xor(s, 16); s += __shfl_xor(s, 32);
    s2 += __shfl_xor(s2, 16); s2 += __shfl_xor(s2, 32);
    ssum[ct] = s; ssq[ct] = s2;
  }
  if (i16 == 0) {
#pragma unroll
    for (int ks = 0; ks < 18; ++ks) { u32x4 one = {0x3f803f80u, 0x3f803f80u, 0x3f803f80u, 0x3f803f80u}; xb[0][ks] = __builtin_bit_cast(s16x8, one); }
  }
  f32x4 acc[7][3];
#pragma unroll
  for (int ft = 0; ft < 7; ++ft)
#pragma unroll
    for (int ct = 0; ct < 3; ++ct) acc[ft][ct] = f32x4{0.f, 0.f, 0.f, 0.f};
  lds_cptr abase[7];
#pragma unroll
  for (int ft = 0; ft < 7; ++ft) {
    int G = 4 * ft + p4; G = G > 26 ? 26 : G;
    const int seg = G / 9, wi = G - seg * 9;
    abase[ft] = (lds_cptr)smem + (8 * g + q4) * K1_ROW + seg * 80 + par * 8 + wi * 8;
  }
#pragma unroll
  for (int st = 0; st < 3; ++st) {
    if (st == 0) WAIT_BAR(24); else if (st == 1) WAIT_BAR(12); else WAIT_BAR(0);
#pragma unroll
    for (int kk = 0; kk < 6; ++kk) {
      const int ks = st * 6 + kk;
#pragma unroll
      for (int ft = 0; ft < 7; ++ft) {
        s16x8 a = cat8(vtr(abase[ft] + ks * 32 * K1_ROW), vtr(abase[ft] + ks * 32 * K1_ROW + 4 * K1_ROW));
#pragma unroll
        for (int ct = 0; ct < 3; ++ct) acc[ft][ct] = MFMA16(a, xb[ct][ks], acc[ft][ct]);
      }
    }
  }
  WAIT_BAR(0);
  lds_ptr qs = (lds_ptr)smem + wid * (48 * K1_QS);
#pragma unroll
  for (int ct = 0; ct < 3; ++ct) {
    const float mean = ssum[ct] * (1.0f / 576.0f);
    float var = ssq[ct] * (1.0f / 576.0f) - mean * mean;
    var = var < 0.f ? 0.f : var;
    const float rs = 1.0f / sqrtf(var + EPS);
    const float sm = rs * mean;
#pragma unroll
    for (int ft = 0; ft < 7; ++ft) {
      const int G = 4 * ft + g;
      float v[4];
#pragma unroll
      for (int r = 0; r < 4; ++r) {
        const float c = __shfl(acc[ft][0][r], lane & 48);
        v[r] = rs * acc[ft][ct][r] - sm * c;
      }
      if (G < 27) {
        u32x2 pk = {pack_bf16(v[0], v[1]), pack_bf16(v[2], v[3])};
        *(__attribute__((address_space(3))) u32x2*)(qs + (ct * 16 + i16) * K1_QS + G * 8) = pk;
      }
    }
  }
  asm volatile("s_waitcnt lgkmcnt(0)" ::: "memory");
  {
    const int tk = lane & 31, hd = lane >> 5;
    lds_cptr qrow = (lds_cptr)qs + (16 + tk) * K1_QS + hd * 36;
    unsigned qv[9];
#pragma unroll
    for (int d = 0; d < 9; ++d) qv[d] = *(__attribute__((address_space(3))) const unsigned*)(qrow + d * 4);
    float lg[8];
#pragma unroll
    for (int w = 0; w < 8; ++w) {
      lds_cptr krow = qrow - 2 * w * K1_QS + 72;
      float a = 0.f;
#pragma unroll
      for (int d = 0; d < 9; ++d) a = dot2bf(qv[d], *(__attribute__((address_space(3))) const unsigned*)(krow + d * 4), a);
      lg[w] = (T0 + tk - 2 * w >= 0) ? a * 0.23570226039551584f : -1e9f;
    }
    float mx = lg[0];
#pragma unroll
    for (int w = 1; w < 8; ++w) mx = fmaxf(mx, lg[w]);
    float se = 0.f;
#pragma unroll
    for (int w = 0; w < 8; ++w) { lg[w] = __builtin_amdgcn_exp2f((lg[w] - mx) * 1.4426950408889634f); se += lg[w]; }
    const float inv = 1.0f / se;
    float ov[18];
#pragma unroll
    for (int d = 0; d < 18; ++d) ov[d] = 0.f;
#pragma unroll
    for (int w = 0; w < 8; ++w) {
      lds_cptr vrow = qrow - 2 * w * K1_QS + 144;
      const float pw = lg[w] * inv;
#pragma unroll
      for (int d = 0; d < 9; ++d) {
        const unsigned u = *(__attribute__((address_space(3))) const unsigned*)(vrow + d * 4);
        ov[2 * d] = __builtin_fmaf(pw, bf_lo(u), ov[2 * d]);
        ov[2 * d + 1] = __builtin_fmaf(pw, bf_hi(u), ov[2 * d + 1]);
      }
    }
    unsigned* op = (unsigned*)(o + (size_t)(T0 + tk) * D_ + hp * 36 + hd * 18);
#pragma unroll
    for (int d = 0; d < 9; ++d) op[d] = pack_bf16(ov[2 * d], ov[2 * d + 1]);
  }
}

constexpr int K2_LDS = 56 * 1024;

__global__ __launch_bounds__(256, 1) void k2_proj(const u16* __restrict__ o, const u16* __restrict__ x,
                                                  const u16* __restrict__ wo, const u16* __restrict__ w1,
                                                  u16* __restrict__ h, float* __restrict__ stats, float* __restrict__ c1) {
  extern __shared__ __attribute__((aligned(16))) char smem[];
  const int tid = threadIdx.x, lane = tid & 63;
  const int b = blockIdx.x;
  if (b >= 192) {
    const int cb = b - 192;
    const int cg = tid % 9, rg = tid / 9;
    float* red = (float*)smem;
    if (rg < 28) {
      float s[8];
#pragma unroll
      for (int e = 0; e < 8; ++e) s[e] = 0.f;
      for (int r = rg; r < 576; r += 28) {
        u32x4 v = *(const u32x4*)(w1 + (size_t)r * F_ + cb * 72 + cg * 8);
#pragma unroll
        for (int e = 0; e < 4; ++e) { s[2 * e] += bf_lo(v[e]); s[2 * e + 1] += bf_hi(v[e]); }
      }
#pragma unroll
      for (int e = 0; e < 8; ++e) red[rg * 72 + cg * 8 + e] = s[e];
    }
    __syncthreads();
    if (tid < 72) {
      float s = 0.f;
      for (int r = 0; r < 28; ++r) s += red[r * 72 + tid];
      c1[cb * 72 + tid] = s;
    }
    return;
  }
  const int wid = __builtin_amdgcn_readfirstlane(tid >> 6);
  const int xcd = b & 7, bi = b >> 3;
  const int pnl = 2 * xcd + (bi >= 12 ? 1 : 0), n = bi >= 12 ? bi - 12 : bi;
  const int i16 = lane & 15, g = lane >> 4, q4 = i16 >> 2, p4 = i16 & 3;
  const unsigned lds0 = (unsigned)(uintptr_t)smem;
  const int T0 = pnl * 128 + wid * 32;

  s16x8 ob[2][18];
#pragma unroll
  for (int ct = 0; ct < 2; ++ct) {
    const u16* orow = o + (size_t)(T0 + ct * 16 + i16) * D_ + 8 * g;
#pragma unroll
    for (int ks = 0; ks < 18; ++ks) ob[ct][ks] = *(const s16x8*)(orow + 32 * ks);
  }
#pragma unroll
  for (int j = 0; j < 14; ++j) {
    int ni = j * 4 + wid; ni = ni > 53 ? 53 : ni;
    const int idx = ni * 64 + lane;
    const int row = idx / 6, ch = idx - row * 6;
    glds16((unsigned)(row * 1152 + n * 96 + ch * 16), wo, lds0 + (unsigned)(ni * 1024));
  }
  u32x2 xr[2][3];
#pragma unroll
  for (int ct = 0; ct < 2; ++ct)
#pragma unroll
    for (int ft = 0; ft < 3; ++ft)
      xr[ct][ft] = *(const u32x2*)(x + (size_t)(T0 + ct * 16 + i16) * D_ + n * 48 + ft * 16 + 4 * g);

  f32x4 acc[3][2];
#pragma unroll
  for (int ft = 0; ft < 3; ++ft)
#pragma unroll
    for (int ct = 0; ct < 2; ++ct) acc[ft][ct] = f32x4{0.f, 0.f, 0.f, 0.f};
  lds_cptr abase = (lds_cptr)smem + (8 * g + q4) * 96 + p4 * 8;
  WAIT_BAR(0);
#pragma unroll
  for (int ks = 0; ks < 18; ++ks)
#pragma unroll
    for (int ft = 0; ft < 3; ++ft) {
      s16x8 a = cat8(vtr(abase + ks * 32 * 96 + ft * 32), vtr(abase + ks * 32 * 96 + ft * 32 + 4 * 96));
#pragma unroll
      for (int ct = 0; ct < 2; ++ct) acc[ft][ct] = MFMA16(a, ob[ct][ks], acc[ft][ct]);
    }
#pragma unroll
  for (int ct = 0; ct < 2; ++ct) {
    const int tok = T0 + ct * 16 + i16;
    float hv[12];
#pragma unroll
    for (int ft = 0; ft < 3; ++ft) {
      const unsigned p0 = pack_bf16(bf_lo(xr[ct][ft][0]) + acc[ft][ct][0], bf_hi(xr[ct][ft][0]) + acc[ft][ct][1]);
      const unsigned p1 = pack_bf16(bf_lo(xr[ct][ft][1]) + acc[ft][ct][2], bf_hi(xr[ct][ft][1]) + acc[ft][ct][3]);
      u32x2 pk = {p0, p1};
      *(u32x2*)(h + (size_t)tok * D_ + n * 48 + ft * 16 + 4 * g) = pk;
      hv[ft * 4 + 0] = bf_lo(p0); hv[ft * 4 + 1] = bf_hi(p0); hv[ft * 4 + 2] = bf_lo(p1); hv[ft * 4 + 3] = bf_hi(p1);
    }
    float s = 0.f;
#pragma unroll
    for (int e = 0; e < 12; ++e) s += hv[e];
    s += __shfl_xor(s, 16); s += __shfl_xor(s, 32);
    const float mean = s * (1.0f / 48.0f);
    float m2 = 0.f;
#pragma unroll
    for (int e = 0; e < 12; ++e) { const float d = hv[e] - mean; m2 = __builtin_fmaf(d, d, m2); }
    m2 += __shfl_xor(m2, 16); m2 += __shfl_xor(m2, 32);
    if (g == 0) { stats[(size_t)tok * 24 + n * 2] = s; stats[(size_t)tok * 24 + n * 2 + 1] = m2; }
  }
}

constexpr int SLOT = 36864;
constexpr int K3_TAB = 4 * SLOT;
constexpr int K3_LDS = K3_TAB + 288 * 8;

__global__ __launch_bounds__(256, 1) void k3_mlp(const u16* __restrict__ h, const float* __restrict__ stats,
                                                 const float* __restrict__ c1, const u16* __restrict__ w1,
                                                 const u16* __restrict__ b1, const u16* __restrict__ w2,
                                                 u32x4* __restrict__ part) {
  extern __shared__ __attribute__((aligned(16))) char smem[];
  const int tid = threadIdx.x, lane = tid & 63;
  const int wid = __builtin_amdgcn_readfirstlane(tid >> 6);
  const int t32 = lane & 31, hh = lane >> 5;
  const int b = blockIdx.x, xcd = b & 7, bi = b >> 3;
  const int c = 2 * xcd + (bi & 1), pnl = bi >> 1;
  const int tok = pnl * 128 + wid * 32 + t32;
  const unsigned lds0 = (unsigned)(uintptr_t)smem;
  const int i16 = lane & 15, q4 = i16 >> 2, p4 = i16 & 3, blk = (lane >> 4) & 1;

  s16x8 hb[36];
  {
    const u16* hr = h + (size_t)tok * D_ + 8 * hh;
#pragma unroll
    for (int ks = 0; ks < 36; ++ks) hb[ks] = *(const s16x8*)(hr + 16 * ks);
  }
  f32x4 stv[6];
#pragma unroll
  for (int i = 0; i < 6; ++i) stv[i] = *(const f32x4*)(stats + (size_t)tok * 24 + i * 4);
  float tabc[2], tabb[2];
  {
    tabc[0] = c1[288 * c + tid]; tabb[0] = bf_lo(b1[288 * c + tid]);
    tabc[1] = 0.f; tabb[1] = 0.f;
    if (tid < 32) { tabc[1] = c1[288 * c + 256 + tid]; tabb[1] = bf_lo(b1[288 * c + 256 + tid]); }
  }
  unsigned off1[9], off2[9];
#pragma unroll
  for (int j = 0; j < 9; ++j) {
    const int idx = (j * 4 + wid) * 64 + lane;
    const int row = idx / 36, c16 = idx - row * 36;
    off1[j] = (unsigned)(row * 9216 + c16 * 16);
    off2[j] = (unsigned)((row & 31) * 1152 + c16 * 16);
  }
  auto issue_slot = [&](auto S) AINL {
    constexpr int s = S;
    const unsigned dst = lds0 + (unsigned)((s & 3) * SLOT + wid * 1024);
    if constexpr (s < 9) {
      const char* base = (const char*)w1 + ((size_t)(64 * s) * F_ + 288 * c) * 2;
#pragma unroll
      for (int j = 0; j < 9; ++j) glds16(off1[j], base, dst + j * 4096);
    } else {
      constexpr int s2 = s - 9;
#pragma unroll
      for (int j = 0; j < 9; ++j) {
        const int half = (4 * j + wid) >= 18 ? 1 : 0;
        const int hs = 2 * s2 + half;
        const int pass = hs >= 9 ? 1 : 0, ft = hs - 9 * pass;
        const char* base = (const char*)w2 + ((size_t)(288 * c + 32 * ft) * D_ + 288 * pass) * 2;
        glds16(off2[j], base, dst + j * 4096);
      }
    }
  };
  issue_slot(std::integral_constant<int, 0>{}); issue_slot(std::integral_constant<int, 1>{}); issue_slot(std::integral_constant<int, 2>{});

  {
    float* tc = (float*)(smem + K3_TAB);
    tc[tid] = tabc[0]; tc[288 + tid] = tabb[0];
    if (tid < 32) { tc[256 + tid] = tabc[1]; tc[288 + 256 + tid] = tabb[1]; }
  }
  float sc, scm;
  {
    float sum = 0.f;
#pragma unroll
    for (int i = 0; i < 6; ++i) sum += stv[i][0] + stv[i][2];
    const float mean = sum * (1.0f / 576.0f);
    float m2 = 0.f;
#pragma unroll
    for (int i = 0; i < 6; ++i) {
      const float d0 = stv[i][0] * (1.0f / 48.0f) - mean, d1 = stv[i][2] * (1.0f / 48.0f) - mean;
      m2 += stv[i][1] + stv[i][3] + 48.0f * (d0 * d0 + d1 * d1);
    }
    const float var = m2 * (1.0f / 576.0f);
    const float r1 = 1.0f / sqrtf(var + EPS);
    const float var2 = var * r1 * r1;
    const float r2 = 1.0f / sqrtf(var2 + EPS);
    sc = r1 * r2; scm = sc * mean;
  }

  lds_cptr base1 = (lds_cptr)smem + (8 * hh + q4) * 576 + (16 * blk + 4 * p4) * 2;
  lds_cptr base2 = (lds_cptr)smem + (4 * hh + q4) * 576 + (16 * blk + 4 * p4) * 2;

  f32x16 acc1[9];
#pragma unroll
  for (int i = 0; i < 9; ++i)
#pragma unroll
    for (int r = 0; r < 16; ++r) acc1[i][r] = 0.f;
  f32x16 acc2[9];
  s16x8 xbf[9][2];

  auto rd = [&](auto N) AINL -> s16x8 {
    constexpr int n = N;
    if constexpr (n < 324) {
      constexpr int s = n / 36, m = n % 36, ft = m / 4, ks = m % 4;
      lds_cptr a = base1 + (s & 3) * SLOT + ks * 16 * 576 + ft * 64;
      return cat8(vtr(a), vtr(a + 4 * 576));
    } else {
      constexpr int n2 = n - 324, hs = n2 / 18, m = n2 % 18, dt = m / 2, s = m % 2;
      constexpr int slot = 9 + hs / 2;
      lds_cptr a = base2 + (slot & 3) * SLOT + (hs & 1) * 18432 + s * 16 * 576 + dt * 64;
      return cat8(vtr(a), vtr(a + 8 * 576));
    }
  };
  auto gelu_tile = [&](auto FT) AINL {
    constexpr int ft = FT;
    lds_cptr tc = (lds_cptr)smem + K3_TAB + (32 * ft + 4 * hh) * 4;
    float gv[16];
#pragma unroll
    for (int rg = 0; rg < 4; ++rg) {
      const f32x4 cc = *(__attribute__((address_space(3))) const f32x4*)(tc + rg * 32);
      const f32x4 bb = *(__attribute__((address_space(3))) const f32x4*)(tc + 288 * 4 + rg * 32);
#pragma unroll
      for (int e = 0; e < 4; ++e) {
        const float v = __builtin_fmaf(sc, acc1[ft][rg * 4 + e], __builtin_fmaf(-scm, cc[e], bb[e]));
        gv[rg * 4 + e] = gelu_tanh(v);
      }
    }
#pragma unroll
    for (int s = 0; s < 2; ++s) {
      u32x4 pk = {pack_bf16(gv[8 * s], gv[8 * s + 1]), pack_bf16(gv[8 * s + 2], gv[8 * s + 3]),
                  pack_bf16(gv[8 * s + 4], gv[8 * s + 5]), pack_bf16(gv[8 * s + 6], gv[8 * s + 7])};
      xbf[ft][s] = __builtin_bit_cast(s16x8, pk);
    }
  };
  auto store_pass = [&](int pass) AINL {
    u32x4* dst = part + ((size_t)(((c * 16 + pnl) * 4 + wid) * 2 + pass) * 18) * 64 + lane;
#pragma unroll
    for (int dt = 0; dt < 9; ++dt)
#pragma unroll
      for (int h2 = 0; h2 < 2; ++h2) {
        u32x4 pk = {pack_bf16(acc2[dt][8 * h2], acc2[dt][8 * h2 + 1]), pack_bf16(acc2[dt][8 * h2 + 2], acc2[dt][8 * h2 + 3]),
                    pack_bf16(acc2[dt][8 * h2 + 4], acc2[dt][8 * h2 + 5]), pack_bf16(acc2[dt][8 * h2 + 6], acc2[dt][8 * h2 + 7])};
        dst[(dt * 2 + h2) * 64] = pk;
      }
  };

  constexpr int LA = 4;
  wait_bar<9>();
  s16x8 fr[LA];
  static_for<0, LA>([&](auto N) AINL { fr[N] = rd(N); });

  static_for<0, 18>([&](auto SS) AINL {
    constexpr int s = SS;
    if constexpr (s + 3 < 18) issue_slot(std::integral_constant<int, s + 3>{});
    static_for<0, 36>([&](auto MM) AINL {
      constexpr int m = MM;
      constexpr int n = s * 36 + m;
      const s16x8 a = fr[n % LA];
      if constexpr (n < 324) {
        constexpr int ft = m / 4, ks = m % 4;
        acc1[ft] = MFMA32(a, hb[4 * s + ks], acc1[ft]);
      } else {
        constexpr int n2 = n - 324, hs = n2 / 18, mm = n2 % 18, dt = mm / 2, ss = mm % 2;
        constexpr int ft = hs % 9;
        if constexpr (mm == 0) {
          if constexpr (hs == 0) gelu_tile(std::integral_constant<int, 0>{});
          if constexpr (ft == 0) {
#pragma unroll
            for (int i = 0; i < 9; ++i)
#pragma unroll
              for (int r = 0; r < 16; ++r) acc2[i][r] = 0.f;
          }
          if constexpr (hs + 1 < 9) gelu_tile(std::integral_constant<int, hs + 1>{});
        }
        acc2[dt] = MFMA32(a, xbf[ft][ss], acc2[dt]);
        if constexpr (mm == 17 && ft == 8) store_pass(hs / 9);
      }
      if constexpr (n + LA < 648) fr[n % LA] = rd(std::integral_constant<int, n + LA>{});
    });
    if constexpr (s < 13) wait_bar<9>();
    else if constexpr (s < 15) wait_bar<27>();
    else if constexpr (s == 15) wait_bar<0>();
  });
}

__global__ __launch_bounds__(256) void k4_reduce(const u32x4* __restrict__ part, const u16* __restrict__ h,
                                                 const u16* __restrict__ b2, u16* __restrict__ out) {
  const int gid = blockIdx.x * 256 + threadIdx.x;
  const int lane = gid & 63, rest = gid >> 6;
  const int h2 = rest & 1, r1 = rest >> 1;
  const int dt = r1 % 9, r2 = r1 / 9;
  const int pass = r2 & 1, r3 = r2 >> 1;
  const int tok = r3 * 32 + (lane & 31), hh = lane >> 5;
  const int d0 = 288 * pass + 32 * dt + 16 * h2 + 4 * hh;
  u32x4 v[16];
#pragma unroll
  for (int c = 0; c < 16; ++c) v[c] = part[(size_t)c * 147456 + gid];
  const u32x2 ha = *(const u32x2*)(h + (size_t)tok * D_ + d0), hb = *(const u32x2*)(h + (size_t)tok * D_ + d0 + 8);
  const u32x2 ba = *(const u32x2*)(b2 + d0), bb = *(const u32x2*)(b2 + d0 + 8);
  float s[8];
  s[0] = bf_lo(ha[0]) + bf_lo(ba[0]); s[1] = bf_hi(ha[0]) + bf_hi(ba[0]);
  s[2] = bf_lo(ha[1]) + bf_lo(ba[1]); s[3] = bf_hi(ha[1]) + bf_hi(ba[1]);
  s[4] = bf_lo(hb[0]) + bf_lo(bb[0]); s[5] = bf_hi(hb[0]) + bf_hi(bb[0]);
  s[6] = bf_lo(hb[1]) + bf_lo(bb[1]); s[7] = bf_hi(hb[1]) + bf_hi(bb[1]);
  float a[8];
#pragma unroll
  for (int e = 0; e < 8; ++e) a[e] = 0.f;
#pragma unroll
  for (int c = 0; c < 16; ++c)
#pragma unroll
    for (int e = 0; e < 4; ++e) { a[2 * e] += bf_lo(v[c][e]); a[2 * e + 1] += bf_hi(v[c][e]); }
  u32x2 oa = {pack_bf16(s[0] + a[0], s[1] + a[1]), pack_bf16(s[2] + a[2], s[3] + a[3])};
  u32x2 ob = {pack_bf16(s[4] + a[4], s[5] + a[5]), pack_bf16(s[6] + a[6], s[7] + a[7])};
  *(u32x2*)(out + (size_t)tok * D_ + d0) = oa;
  *(u32x2*)(out + (size_t)tok * D_ + d0 + 8) = ob;
}

extern "C" void kernel_launch(void* const* d_in, const int* in_sizes, int n_in, void* d_out, int out_size,
                              void* d_ws, size_t ws_size, hipStream_t stream) {
  const u16* x = (const u16*)d_in[0];
  const u16* wqkv = (const u16*)d_in[1];
  const u16* wo = (const u16*)d_in[2];
  const u16* w1 = (const u16*)d_in[4];
  const u16* b1 = (const u16*)d_in[5];
  const u16* w2 = (const u16*)d_in[6];
  const u16* b2 = (const u16*)d_in[7];
  u16* out = (u16*)d_out;
  char* ws = (char*)d_ws;
  u16* o = (u16*)ws;
  u16* h = (u16*)(ws + 2359296);
  float* stats = (float*)(ws + 2 * 2359296);
  float* c1 = (float*)(ws + 2 * 2359296 + 196608);
  u32x4* part = (u32x4*)(ws + 2 * 2359296 + 196608 + 18432 + 2048);

  hipFuncSetAttribute((const void*)k1_qkv_attn, hipFuncAttributeMaxDynamicSharedMemorySize, K1_LDS);
  hipFuncSetAttribute((const void*)k2_proj, hipFuncAttributeMaxDynamicSharedMemorySize, K2_LDS);
  hipFuncSetAttribute((const void*)k3_mlp, hipFuncAttributeMaxDynamicSharedMemorySize, K3_LDS);

  k1_qkv_attn<<<256, 256, K1_LDS, stream>>>(x, wqkv, o);
  k2_proj<<<256, 256, K2_LDS, stream>>>(o, x, wo, w1, h, stats, c1);
  k3_mlp<<<256, 256, K3_LDS, stream>>>(h, stats, c1, w1, b1, w2, part);
  k4_reduce<<<576, 256, 0, stream>>>(part, h, b2, out);
}
```

```cpp
#include <hip/hip_runtime.h>
#include <hip/hip_bf16.h>
#include <stdint.h>
#include <type_traits>

typedef short s16x4 __attribute__((ext_vector_type(4)));
typedef short s16x8 __attribute__((ext_vector_type(8)));
typedef __bf16 bf2_t __attribute__((ext_vector_type(2)));
typedef float f32x4 __attribute__((ext_vector_type(4)));
typedef float f32x16 __attribute__((ext_vector_type(16)));
typedef unsigned u32x4 __attribute__((ext_vector_type(4)));
typedef unsigned u32x2 __attribute__((ext_vector_type(2)));
typedef unsigned short u16;
typedef __attribute__((address_space(3))) const char* lds_cptr;
typedef __attribute__((address_space(3))) char* lds_ptr;
typedef __attribute__((address_space(3))) s16x4* lds_s16x4_ptr;

#define DEV __device__ __forceinline__
#define MFMA32(a, b, c) __builtin_amdgcn_mfma_f32_32x32x16_bf16(a, b, c, 0, 0, 0)
#define MFMA16(a, b, c) __builtin_amdgcn_mfma_f32_16x16x32_bf16(a, b, c, 0, 0, 0)

constexpr int S_ = 2048, D_ = 576, F_ = 4608;
constexpr float EPS = 1e-5f;

DEV s16x4 vtr(lds_cptr p) { return __builtin_amdgcn_ds_read_tr16_b64_v4i16((lds_s16x4_ptr)p); }
DEV s16x8 cat8(s16x4 lo, s16x4 hi) { return __builtin_shufflevector(lo, hi, 0, 1, 2, 3, 4, 5, 6, 7); }
DEV unsigned pack_bf16(float a, float b) {
  bf2_t v; v[0] = (__bf16)a; v[1] = (__bf16)b; return __builtin_bit_cast(unsigned, v);
}
DEV float bf_lo(unsigned u) { return __builtin_bit_cast(float, u << 16); }
DEV float bf_hi(unsigned u) { return __builtin_bit_cast(float, u & 0xffff0000u); }
DEV float dot2bf(unsigned a, unsigned b, float c) {
  return __builtin_amdgcn_fdot2_f32_bf16(__builtin_bit_cast(bf2_t, a), __builtin_bit_cast(bf2_t, b), c, false);
}
DEV void glds16(unsigned voff, const void* sbase_, unsigned ldsaddr_) {
  unsigned sv;
  const uint64_t pv = (uint64_t)sbase_;
  const uint64_t sbase = ((uint64_t)(unsigned)__builtin_amdgcn_readfirstlane((int)(pv >> 32)) << 32) |
                         (uint64_t)(unsigned)__builtin_amdgcn_readfirstlane((int)(unsigned)pv);
  const unsigned ldsaddr = (unsigned)__builtin_amdgcn_readfirstlane((int)ldsaddr_);
  asm volatile("s_mov_b32 %0, m0\n\ts_mov_b32 m0, %3\n\ts_nop 0\n\tglobal_load_lds_dwordx4 %1, %2\n\ts_mov_b32 m0, %0"
               : "=&s"(sv) : "v"(voff), "s"(sbase), "s"(ldsaddr) : "memory");
}
DEV void gtouch(unsigned voff, const void* sbase_, unsigned ldsaddr_) {
  unsigned sv;
  const uint64_t pv = (uint64_t)sbase_;
  const uint64_t sbase = ((uint64_t)(unsigned)__builtin_amdgcn_readfirstlane((int)(pv >> 32)) << 32) |
                         (uint64_t)(unsigned)__builtin_amdgcn_readfirstlane((int)(unsigned)pv);
  const unsigned ldsaddr = (unsigned)__builtin_amdgcn_readfirstlane((int)ldsaddr_);
  asm volatile("s_mov_b32 %0, m0\n\ts_mov_b32 m0, %3\n\ts_nop 0\n\tglobal_load_lds_dword %1, %2\n\ts_mov_b32 m0, %0"
               : "=&s"(sv) : "v"(voff), "s"(sbase), "s"(ldsaddr) : "memory");
}
#define WAIT_BAR(N) asm volatile("s_waitcnt vmcnt(" #N ") lgkmcnt(0)\n\ts_barrier" ::: "memory")

template <int VM, int LGKM = 15> DEV void waitcnt() {
  __builtin_amdgcn_s_waitcnt((VM & 15) | ((VM >> 4) << 14) | (7 << 4) | ((LGKM & 15) << 8));
}
DEV void barrier() { asm volatile("s_barrier" ::: "memory"); }
template <int N> DEV void wait_bar() { asm volatile("s_waitcnt vmcnt(%0) lgkmcnt(0)\n\ts_barrier" ::"i"(N) : "memory"); }
template <int B, int E, class F> DEV void static_for(F&& f) {
  if constexpr (B < E) { f(std::integral_constant<int, B>{}); static_for<B + 1, E>(f); }
}
#define AINL __attribute__((always_inline))

#define STAMP(k, i) do { } while (0)
DEV float gelu_tanh(float x) {
  const float c1 = -2.0f * 1.4426950408889634f * 0.7978845608028654f;
  const float c3 = c1 * 0.044715f;
  float x2 = x * x;
  float t = x * __builtin_fmaf(x2, c3, c1);
  float e = __builtin_amdgcn_exp2f(t);
  return x * __builtin_amdgcn_rcpf(1.0f + e);
}

constexpr int K1_ROW = 240;
constexpr int K1_W = 576 * K1_ROW;
constexpr int K1_LDS = K1_W + 1024;
constexpr int K1_QS = 232;

__global__ __launch_bounds__(256, 1) void k1_qkv_attn(const u16* __restrict__ x, const u16* __restrict__ wqkv,
                                                      u16* __restrict__ o) {
  extern __shared__ __attribute__((aligned(16))) char smem[];
  const int tid = threadIdx.x, lane = tid & 63;
  const int wid = __builtin_amdgcn_readfirstlane(tid >> 6);
  const int b = blockIdx.x, xcd = b & 7, bi = b >> 3;
  const int hp = 2 * xcd + (bi & 1), pnl = bi >> 1;
  const int par = hp & 1;
  const int i16 = lane & 15, g = lane >> 4, q4 = i16 >> 2, p4 = i16 & 3;
  const unsigned lds0 = (unsigned)(uintptr_t)smem;
  const int T0 = pnl * 128 + wid * 32;

  s16x8 xb[3][18];
#pragma unroll
  for (int ct = 0; ct < 3; ++ct) {
    int tok = T0 - 16 + ct * 16 + i16;
    tok = tok < 0 ? 0 : tok;
    const u16* xr = x + (size_t)tok * D_ + 8 * g;
#pragma unroll
    for (int ks = 0; ks < 18; ++ks) xb[ct][ks] = *(const s16x8*)(xr + 32 * ks);
  }
  {
    const int segbase = (72 * hp) & ~15;
#pragma unroll
    for (int st = 0; st < 3; ++st) {
#pragma unroll
      for (int j = 0; j < 12; ++j) {
        int n = j * 4 + wid;
        n = n > 44 ? 44 : n;
        const int idx = (st * 45 + n) * 64 + lane;
        const int row = idx / 15, ch = idx - row * 15;
        const int seg = ch / 5, cc = ch - seg * 5;
        const unsigned voff = (unsigned)(row * 3456 + seg * 1152 + segbase + cc * 16);
        glds16(voff, wqkv, lds0 + (unsigned)((st * 45 + n) * 1024));
      }
    }
  }
  float ssum[3], ssq[3];
#pragma unroll
  for (int ct = 0; ct < 3; ++ct) {
    float s = 0.f, s2 = 0.f;
#pragma unroll
    for (int ks = 0; ks < 18; ++ks) {
      u32x4 v = __builtin_bit_cast(u32x4, xb[ct][ks]);
#pragma unroll
      for (int e = 0; e < 4; ++e) { s = dot2bf(v[e], 0x3f803f80u, s); s2 = dot2bf(v[e], v[e], s2); }
    }
    s += __shfl_xor(s, 16); s += __shfl_xor(s, 32);
    s2 += __shfl_xor(s2, 16); s2 += __shfl_xor(s2, 32);
    ssum[ct] = s; ssq[ct] = s2;
  }
  if (i16 == 0) {
#pragma unroll
    for (int ks = 0; ks < 18; ++ks) { u32x4 one = {0x3f803f80u, 0x3f803f80u, 0x3f803f80u, 0x3f803f80u}; xb[0][ks] = __builtin_bit_cast(s16x8, one); }
  }
  f32x4 acc[7][3];
#pragma unroll
  for (int ft = 0; ft < 7; ++ft)
#pragma unroll
    for (int ct = 0; ct < 3; ++ct) acc[ft][ct] = f32x4{0.f, 0.f, 0.f, 0.f};
  lds_cptr abase[7];
#pragma unroll
  for (int ft = 0; ft < 7; ++ft) {
    int G = 4 * ft + p4; G = G > 26 ? 26 : G;
    const int seg = G / 9, wi = G - seg * 9;
    abase[ft] = (lds_cptr)smem + (8 * g + q4) * K1_ROW + seg * 80 + par * 8 + wi * 8;
  }
#pragma unroll
  for (int st = 0; st < 3; ++st) {
    if (st == 0) WAIT_BAR(24); else if (st == 1) WAIT_BAR(12); else WAIT_BAR(0);
#pragma unroll
    for (int kk = 0; kk < 6; ++kk) {
      const int ks = st * 6 + kk;
#pragma unroll
      for (int ft = 0; ft < 7; ++ft) {
        s16x8 a = cat8(vtr(abase[ft] + ks * 32 * K1_ROW), vtr(abase[ft] + ks * 32 * K1_ROW + 4 * K1_ROW));
#pragma unroll
        for (int ct = 0; ct < 3; ++ct) acc[ft][ct] = MFMA16(a, xb[ct][ks], acc[ft][ct]);
      }
    }
  }
  WAIT_BAR(0);
  lds_ptr qs = (lds_ptr)smem + wid * (48 * K1_QS);
#pragma unroll
  for (int ct = 0; ct < 3; ++ct) {
    const float mean = ssum[ct] * (1.0f / 576.0f);
    float var = ssq[ct] * (1.0f / 576.0f) - mean * mean;
    var = var < 0.f ? 0.f : var;
    const float rs = 1.0f / sqrtf(var + EPS);
    const float sm = rs * mean;
#pragma unroll
    for (int ft = 0; ft < 7; ++ft) {
      const int G = 4 * ft + g;
      float v[4];
#pragma unroll
      for (int r = 0; r < 4; ++r) {
        const float c = __shfl(acc[ft][0][r], lane & 48);
        v[r] = rs * acc[ft][ct][r] - sm * c;
      }
      if (G < 27) {
        u32x2 pk = {pack_bf16(v[0], v[1]), pack_bf16(v[2], v[3])};
        *(__attribute__((address_space(3))) u32x2*)(qs + (ct * 16 + i16) * K1_QS + G * 8) = pk;
      }
    }
  }
  asm volatile("s_waitcnt lgkmcnt(0)" ::: "memory");
  {
    const int tk = lane & 31, hd = lane >> 5;
    lds_cptr qrow = (lds_cptr)qs + (16 + tk) * K1_QS + hd * 36;
    unsigned qv[9];
#pragma unroll
    for (int d = 0; d < 9; ++d) qv[d] = *(__attribute__((address_space(3))) const unsigned*)(qrow + d * 4);
    float lg[8];
#pragma unroll
    for (int w = 0; w < 8; ++w) {
      lds_cptr krow = qrow - 2 * w * K1_QS + 72;
      float a = 0.f;
#pragma unroll
      for (int d = 0; d < 9; ++d) a = dot2bf(qv[d], *(__attribute__((address_space(3))) const unsigned*)(krow + d * 4), a);
      lg[w] = (T0 + tk - 2 * w >= 0) ? a * 0.23570226039551584f : -1e9f;
    }
    float mx = lg[0];
#pragma unroll
    for (int w = 1; w < 8; ++w) mx = fmaxf(mx, lg[w]);
    float se = 0.f;
#pragma unroll
    for (int w = 0; w < 8; ++w) { lg[w] = __builtin_amdgcn_exp2f((lg[w] - mx) * 1.4426950408889634f); se += lg[w]; }
    const float inv = 1.0f / se;
    float ov[18];
#pragma unroll
    for (int d = 0; d < 18; ++d) ov[d] = 0.f;
#pragma unroll
    for (int w = 0; w < 8; ++w) {
      lds_cptr vrow = qrow - 2 * w * K1_QS + 144;
      const float pw = lg[w] * inv;
#pragma unroll
      for (int d = 0; d < 9; ++d) {
        const unsigned u = *(__attribute__((address_space(3))) const unsigned*)(vrow + d * 4);
        ov[2 * d] = __builtin_fmaf(pw, bf_lo(u), ov[2 * d]);
        ov[2 * d + 1] = __builtin_fmaf(pw, bf_hi(u), ov[2 * d + 1]);
      }
    }
    unsigned* op = (unsigned*)(o + (size_t)(T0 + tk) * D_ + hp * 36 + hd * 18);
#pragma unroll
    for (int d = 0; d < 9; ++d) op[d] = pack_bf16(ov[2 * d], ov[2 * d + 1]);
  }
}

constexpr int K2_LDS = 56 * 1024;

__global__ __launch_bounds__(256, 1) void k2_proj(const u16* __restrict__ o, const u16* __restrict__ x,
                                                  const u16* __restrict__ wo, const u16* __restrict__ w1,
                                                  u16* __restrict__ h, float* __restrict__ stats, float* __restrict__ c1) {
  extern __shared__ __attribute__((aligned(16))) char smem[];
  const int tid = threadIdx.x, lane = tid & 63;
  const int b = blockIdx.x;
  if (b >= 192) {
    const int cb = b - 192;
    const int cg = tid % 9, rg = tid / 9;
    float* red = (float*)smem;
    if (rg < 28) {
      float s[8];
#pragma unroll
      for (int e = 0; e < 8; ++e) s[e] = 0.f;
      for (int r = rg; r < 576; r += 28) {
        u32x4 v = *(const u32x4*)(w1 + (size_t)r * F_ + cb * 72 + cg * 8);
#pragma unroll
        for (int e = 0; e < 4; ++e) { s[2 * e] += bf_lo(v[e]); s[2 * e + 1] += bf_hi(v[e]); }
      }
#pragma unroll
      for (int e = 0; e < 8; ++e) red[rg * 72 + cg * 8 + e] = s[e];
    }
    __syncthreads();
    if (tid < 72) {
      float s = 0.f;
      for (int r = 0; r < 28; ++r) s += red[r * 72 + tid];
      c1[cb * 72 + tid] = s;
    }
    return;
  }
  const int wid = __builtin_amdgcn_readfirstlane(tid >> 6);
  const int xcd = b & 7, bi = b >> 3;
  const int pnl = 2 * xcd + (bi >= 12 ? 1 : 0), n = bi >= 12 ? bi - 12 : bi;
  const int i16 = lane & 15, g = lane >> 4, q4 = i16 >> 2, p4 = i16 & 3;
  const unsigned lds0 = (unsigned)(uintptr_t)smem;
  const int T0 = pnl * 128 + wid * 32;

  s16x8 ob[2][18];
#pragma unroll
  for (int ct = 0; ct < 2; ++ct) {
    const u16* orow = o + (size_t)(T0 + ct * 16 + i16) * D_ + 8 * g;
#pragma unroll
    for (int ks = 0; ks < 18; ++ks) ob[ct][ks] = *(const s16x8*)(orow + 32 * ks);
  }
#pragma unroll
  for (int j = 0; j < 14; ++j) {
    int ni = j * 4 + wid; ni = ni > 53 ? 53 : ni;
    const int idx = ni * 64 + lane;
    const int row = idx / 6, ch = idx - row * 6;
    glds16((unsigned)(row * 1152 + n * 96 + ch * 16), wo, lds0 + (unsigned)(ni * 1024));
  }
  u32x2 xr[2][3];
#pragma unroll
  for (int ct = 0; ct < 2; ++ct)
#pragma unroll
    for (int ft = 0; ft < 3; ++ft)
      xr[ct][ft] = *(const u32x2*)(x + (size_t)(T0 + ct * 16 + i16) * D_ + n * 48 + ft * 16 + 4 * g);

  f32x4 acc[3][2];
#pragma unroll
  for (int ft = 0; ft < 3; ++ft)
#pragma unroll
    for (int ct = 0; ct < 2; ++ct) acc[ft][ct] = f32x4{0.f, 0.f, 0.f, 0.f};
  lds_cptr abase = (lds_cptr)smem + (8 * g + q4) * 96 + p4 * 8;
  WAIT_BAR(0);
#pragma unroll
  for (int ks = 0; ks < 18; ++ks)
#pragma unroll
    for (int ft = 0; ft < 3; ++ft) {
      s16x8 a = cat8(vtr(abase + ks * 32 * 96 + ft * 32), vtr(abase + ks * 32 * 96 + ft * 32 + 4 * 96));
#pragma unroll
      for (int ct = 0; ct < 2; ++ct) acc[ft][ct] = MFMA16(a, ob[ct][ks], acc[ft][ct]);
    }
#pragma unroll
  for (int ct = 0; ct < 2; ++ct) {
    const int tok = T0 + ct * 16 + i16;
    float hv[12];
#pragma unroll
    for (int ft = 0; ft < 3; ++ft) {
      const unsigned p0 = pack_bf16(bf_lo(xr[ct][ft][0]) + acc[ft][ct][0], bf_hi(xr[ct][ft][0]) + acc[ft][ct][1]);
      const unsigned p1 = pack_bf16(bf_lo(xr[ct][ft][1]) + acc[ft][ct][2], bf_hi(xr[ct][ft][1]) + acc[ft][ct][3]);
      u32x2 pk = {p0, p1};
      {
        const int col = n * 48 + ft * 16 + 4 * g;
        const int ks = col >> 4, hh2 = (col >> 3) & 1, j4 = col & 4;
        *(u32x2*)(h + ((size_t)(((tok >> 5) * 36 + ks) * 64 + hh2 * 32 + (tok & 31)) * 8 + j4)) = pk;
      }
      hv[ft * 4 + 0] = bf_lo(p0); hv[ft * 4 + 1] = bf_hi(p0); hv[ft * 4 + 2] = bf_lo(p1); hv[ft * 4 + 3] = bf_hi(p1);
    }
    float s = 0.f;
#pragma unroll
    for (int e = 0; e < 12; ++e) s += hv[e];
    s += __shfl_xor(s, 16); s += __shfl_xor(s, 32);
    const float mean = s * (1.0f / 48.0f);
    float m2 = 0.f;
#pragma unroll
    for (int e = 0; e < 12; ++e) { const float d = hv[e] - mean; m2 = __builtin_fmaf(d, d, m2); }
    m2 += __shfl_xor(m2, 16); m2 += __shfl_xor(m2, 32);
    if (g == 0) { stats[(size_t)tok * 24 + n * 2] = s; stats[(size_t)tok * 24 + n * 2 + 1] = m2; }
  }
}

constexpr int HS = 18432;
constexpr int K3_XR = 4 * HS;
constexpr int K3_TAB = K3_XR + 73728;
constexpr int K3_SCR = K3_TAB + 288 * 8;
constexpr int K3_LDS = K3_SCR + 256;

__global__ __launch_bounds__(512, 2) void k3_mlp(const u32x4* __restrict__ hT, const float* __restrict__ stats,
                                                 const float* __restrict__ c1, const u16* __restrict__ w1,
                                                 const u16* __restrict__ b1, const u16* __restrict__ w2,
                                                 u32x4* __restrict__ part) {
  extern __shared__ __attribute__((aligned(16))) char smem[];
  const int tid = threadIdx.x, lane = tid & 63;
  const int wid = __builtin_amdgcn_readfirstlane(tid >> 6);
  const int b = blockIdx.x, xcd = b & 7, bi = b >> 3;
  const int c = 2 * xcd + (bi & 1), pnl = bi >> 1;
  const unsigned lds0 = (unsigned)(uintptr_t)smem;
  STAMP(2, 0);

  if (wid >= 4) {
    const int lw = wid - 4;
    unsigned o1[5], o2[5];
#pragma unroll
    for (int j = 0; j < 5; ++j) {
      const int n = j < 4 ? lw + 4 * j : 16 + (lw & 1);
      const int row = n * 16 + (lane >> 2), ch = lane & 3;
      o1[j] = (unsigned)(row * (F_ * 2) + ch * 16);
      o2[j] = (unsigned)(row * (D_ * 2) + ch * 16);
    }
    const char* w1c = (const char*)w1 + (size_t)(288 * c) * 2;
    const char* w2c = (const char*)w2 + (size_t)(288 * c) * D_ * 2;
    auto issue_step = [&](int s) AINL {
      const bool is2 = s >= 18;
      const char* base = is2 ? w2c + (size_t)(64 * (s - 18)) : w1c + ((size_t)(288 * (s & 1)) * F_ + 32 * (s >> 1)) * 2;
      const unsigned dst = lds0 + (unsigned)((s & 3) * HS);
#pragma unroll
      for (int j = 0; j < 4; ++j) glds16(is2 ? o2[j] : o1[j], base, dst + (unsigned)((lw + 4 * j) * 1024));
      if ((lw >> 1) == (s & 1)) glds16(is2 ? o2[4] : o1[4], base, dst + (unsigned)((16 + (lw & 1)) * 1024));
    };
    issue_step(0); issue_step(1); issue_step(2);
    {
      float* tc = (float*)(smem + K3_TAB);
      const int t = tid - 256;
      tc[t] = c1[288 * c + t]; tc[288 + t] = bf_lo(b1[288 * c + t]);
      if (t < 32) { tc[256 + t] = c1[288 * c + 256 + t]; tc[288 + 256 + t] = bf_lo(b1[288 * c + 256 + t]); }
    }
    waitcnt<4, 0>(); barrier();
#pragma unroll 1
    for (int s = 0; s < 36; ++s) {
      if (s + 3 < 36) issue_step(s + 3);
      if (s >= 20) {
        const int d = s - 20;
        lds_cptr sp = (lds_cptr)smem + K3_XR + lw * 18432 + (d & 1) * 2048 + lane * 16;
        const u32x4 q0 = *(__attribute__((address_space(3))) const u32x4*)sp;
        const u32x4 q1 = *(__attribute__((address_space(3))) const u32x4*)(sp + 1024);
        u32x4* dq = part + (size_t)((((c * 16 + pnl) * 4 + lw) * 36) + 2 * d) * 64 + lane;
        dq[0] = q0; dq[64] = q1;
        if (s + 3 < 36) waitcnt<6>(); else waitcnt<2>();
      } else {
        if (s + 3 < 36) waitcnt<4>(); else waitcnt<0>();
      }
      barrier();
    }
    {
      lds_cptr sp = (lds_cptr)smem + K3_XR + lw * 18432 + lane * 16;
      const u32x4 q0 = *(__attribute__((address_space(3))) const u32x4*)sp;
      const u32x4 q1 = *(__attribute__((address_space(3))) const u32x4*)(sp + 1024);
      u32x4* dq = part + (size_t)((((c * 16 + pnl) * 4 + lw) * 36) + 2 * 16) * 64 + lane;
      dq[0] = q0; dq[64] = q1;
    }
    return;
  }

  const int t32 = lane & 31, hh = lane >> 5;
  const int tok = pnl * 128 + wid * 32 + t32;
  const int i16 = lane & 15, q4 = i16 >> 2, p4 = i16 & 3, blk = (lane >> 4) & 1;
  s16x8 hb[36];
  {
    const u32x4* hp = hT + (size_t)((pnl * 4 + wid) * 36) * 64 + lane;
#pragma unroll
    for (int ks = 0; ks < 36; ++ks) hb[ks] = __builtin_bit_cast(s16x8, hp[ks * 64]);
  }
  float sc, scm;
  {
    f32x4 stv[6];
#pragma unroll
    for (int i = 0; i < 6; ++i) stv[i] = *(const f32x4*)(stats + (size_t)tok * 24 + i * 4);
    float sum = 0.f;
#pragma unroll
    for (int i = 0; i < 6; ++i) sum += stv[i][0] + stv[i][2];
    const float mean = sum * (1.0f / 576.0f);
    float m2 = 0.f;
#pragma unroll
    for (int i = 0; i < 6; ++i) {
      const float d0 = stv[i][0] * (1.0f / 48.0f) - mean, d1 = stv[i][2] * (1.0f / 48.0f) - mean;
      m2 += stv[i][1] + stv[i][3] + 48.0f * (d0 * d0 + d1 * d1);
    }
    const float var = m2 * (1.0f / 576.0f);
    const float r1 = __builtin_amdgcn_rsqf(var + EPS);
    const float var2 = var * r1 * r1;
    const float r2 = __builtin_amdgcn_rsqf(var2 + EPS);
    sc = r1 * r2; scm = sc * mean;
  }
  const int lch = (16 * blk + 4 * p4) * 2;
  lds_cptr lb1 = (lds_cptr)smem + (8 * hh + q4) * 64 + lch;
  lds_cptr lb2 = (lds_cptr)smem + (4 * hh + q4) * 64 + lch;
  lds_ptr xw = (lds_ptr)smem + K3_XR + wid * 18432 + lane * 16;

  constexpr int LA = 4;
  s16x8 fr[LA];
  f32x16 accA, accB, zero16;
#pragma unroll
  for (int r = 0; r < 16; ++r) { accA[r] = 0.f; accB[r] = 0.f; zero16[r] = 0.f; }
  using TT = std::true_type; using FF = std::false_type;
  using P0 = std::integral_constant<int, 0>; using P2 = std::integral_constant<int, 2>;

  STAMP(2, 1);
  waitcnt<0, 0>(); barrier();
  STAMP(2, 2);
  static_for<0, LA>([&](auto M) AINL {
    constexpr int m = M;
    lds_cptr a = lb1 + m * 16 * 64;
    fr[m] = cat8(vtr(a), vtr(a + 4 * 64));
  });

  auto pass1 = [&](auto LAST_, f32x16& acc, const f32x16& accp, int it) AINL {
    constexpr bool LAST = decltype(LAST_)::value;
    const int ftp = it == 0 ? 0 : it - 1;
    const int oA = ((2 * it) & 3) * HS, oN = ((2 * it + 2) & 3) * HS;
    unsigned gp[8];
    float v0 = 0.f, v1 = 0.f, u0 = 0.f, u1 = 0.f;
    f32x4 tcc, tbb;
    static_for<0, 36>([&](auto MM) AINL {
      constexpr int m = MM;
      if constexpr (m == 18) barrier();
      acc = MFMA32(fr[m % LA], hb[m], m == 0 ? zero16 : acc);
      constexpr int mn = m + LA;
      lds_cptr a = mn < 36 ? lb1 + oA + (mn / 18) * HS + (mn % 18) * 16 * 64 : (LAST ? lb2 : lb1) + oN + (mn - 36) * 16 * 64;
      constexpr int r2 = (mn >= 36 && LAST) ? 8 * 64 : 4 * 64;
      s16x4 flo, fhi;
      constexpr int ph = (m - 4) & 3, e = ((m - 4) >> 2) * 2;
      const float gc1 = -2.0f * 1.4426950408889634f * 0.7978845608028654f, gc3 = gc1 * 0.044715f;
      if constexpr (m >= 4 && ph == 0) {
        u0 = __builtin_fmaf(-scm, tcc[e & 3], tbb[e & 3]); u1 = __builtin_fmaf(-scm, tcc[(e & 3) + 1], tbb[(e & 3) + 1]);
        flo = vtr(a);
        v0 = __builtin_fmaf(sc, accp[e], u0); v1 = __builtin_fmaf(sc, accp[e + 1], u1);
        fhi = vtr(a + r2);
        u0 = v0 * v0;
      } else if constexpr (m >= 4 && ph == 1) {
        u1 = v1 * v1; u0 = __builtin_fmaf(u0, gc3, gc1);
        flo = vtr(a);
        u1 = __builtin_fmaf(u1, gc3, gc1); u0 = v0 * u0;
        fhi = vtr(a + r2);
        u1 = v1 * u1;
      } else if constexpr (m >= 4 && ph == 2) {
        u0 = __builtin_amdgcn_exp2f(u0); u1 = __builtin_amdgcn_exp2f(u1);
        flo = vtr(a); fhi = vtr(a + r2);
        u0 = 1.0f + u0; u1 = 1.0f + u1;
      } else if constexpr (m >= 4 && ph == 3) {
        u0 = __builtin_amdgcn_rcpf(u0); u1 = __builtin_amdgcn_rcpf(u1);
        flo = vtr(a); fhi = vtr(a + r2);
        gp[e >> 1] = pack_bf16(v0 * u0, v1 * u1);
        asm volatile("" : "+v"(gp[e >> 1]));
      } else { flo = vtr(a); fhi = vtr(a + r2); }
      asm volatile("" : "+v"(v0), "+v"(v1), "+v"(u0), "+v"(u1));
      if constexpr ((m & 7) == 1 && m < 32) {
        lds_cptr tc = (lds_cptr)smem + K3_TAB + (32 * ftp + 4 * hh + m - 1) * 4;
        tcc = *(__attribute__((address_space(3))) const f32x4*)tc;
        tbb = *(__attribute__((address_space(3))) const f32x4*)(tc + 288 * 4);
      }
      if constexpr (m == 20) *(__attribute__((address_space(3))) u32x4*)(xw + (ftp * 2) * 1024) = u32x4{gp[0], gp[1], gp[2], gp[3]};
      fr[m % LA] = cat8(flo, fhi);
      __builtin_amdgcn_sched_barrier(0);
    });
    *(__attribute__((address_space(3))) u32x4*)(xw + (ftp * 2 + 1) * 1024) = u32x4{gp[4], gp[5], gp[6], gp[7]};
    barrier();
  };
#pragma unroll 1
  for (int i2 = 0; i2 < 4; ++i2) {
    pass1(FF{}, accA, accB, 2 * i2);
    pass1(FF{}, accB, accA, 2 * i2 + 1);
  }
  pass1(TT{}, accA, accB, 8);
  STAMP(2, 3);
  {
    float gv[16];
#pragma unroll
    for (int rg = 0; rg < 4; ++rg) {
      lds_cptr tc = (lds_cptr)smem + K3_TAB + (32 * 8 + 4 * hh + 8 * rg) * 4;
      const f32x4 cc = *(__attribute__((address_space(3))) const f32x4*)tc;
      const f32x4 bb = *(__attribute__((address_space(3))) const f32x4*)(tc + 288 * 4);
#pragma unroll
      for (int e = 0; e < 4; ++e) gv[rg * 4 + e] = gelu_tanh(__builtin_fmaf(sc, accA[rg * 4 + e], __builtin_fmaf(-scm, cc[e], bb[e])));
    }
#pragma unroll
    for (int s = 0; s < 2; ++s) {
      u32x4 pk = {pack_bf16(gv[8 * s], gv[8 * s + 1]), pack_bf16(gv[8 * s + 2], gv[8 * s + 3]),
                  pack_bf16(gv[8 * s + 4], gv[8 * s + 5]), pack_bf16(gv[8 * s + 6], gv[8 * s + 7])};
      *(__attribute__((address_space(3))) u32x4*)(xw + (16 + s) * 1024) = pk;
    }
  }
  s16x8 xbf[18];
#pragma unroll
  for (int k = 0; k < 18; ++k) xbf[k] = *(__attribute__((address_space(3))) const s16x8*)(xw + k * 1024);
  STAMP(2, 4);

  u32x4* dst = part + (size_t)(((c * 16 + pnl) * 4 + wid) * 36) * 64 + lane;
  auto pass2 = [&](auto LAST_, auto PH_, f32x16& acc, const f32x16& accp, int d) AINL {
    constexpr bool LAST = decltype(LAST_)::value;
    constexpr int PH = decltype(PH_)::value;
    const int oA = ((18 + d) & 3) * HS, oN = ((19 + d) & 3) * HS;
    lds_ptr sw = xw + ((d + 1) & 1) * 2048;
    u32x4 pk0, pk1;
    static_for<0, 18>([&](auto MM) AINL {
      constexpr int m = MM;
      acc = MFMA32(fr[(m + PH) % LA], xbf[m], m == 0 ? zero16 : acc);
      constexpr int mn = m + LA;
      if constexpr (mn < 18) { lds_cptr a = lb2 + oA + mn * 16 * 64; fr[(m + PH) % LA] = cat8(vtr(a), vtr(a + 8 * 64)); }
      else if constexpr (!LAST) { lds_cptr a = lb2 + oN + (mn - 18) * 16 * 64; fr[(m + PH) % LA] = cat8(vtr(a), vtr(a + 8 * 64)); }
      if constexpr (m >= 2 && m < 6) { pk0[m - 2] = pack_bf16(accp[2 * (m - 2)], accp[2 * (m - 2) + 1]); pk1[m - 2] = pack_bf16(accp[8 + 2 * (m - 2)], accp[8 + 2 * (m - 2) + 1]); }
      if constexpr (m == 6) *(__attribute__((address_space(3))) u32x4*)sw = pk0;
      if constexpr (m == 7) *(__attribute__((address_space(3))) u32x4*)(sw + 1024) = pk1;
      __builtin_amdgcn_sched_barrier(0);
    });
    if constexpr (!LAST) barrier();
  };
#pragma unroll 1
  for (int d2 = 0; d2 < 8; ++d2) {
    pass2(FF{}, P0{}, accA, accB, 2 * d2);
    pass2(FF{}, P2{}, accB, accA, 2 * d2 + 1);
  }
  pass2(FF{}, P0{}, accA, accB, 16);
  pass2(TT{}, P2{}, accB, accA, 17);
  barrier();
#pragma unroll
  for (int h2 = 0; h2 < 2; ++h2) {
    u32x4 pk = {pack_bf16(accB[8 * h2], accB[8 * h2 + 1]), pack_bf16(accB[8 * h2 + 2], accB[8 * h2 + 3]),
                pack_bf16(accB[8 * h2 + 4], accB[8 * h2 + 5]), pack_bf16(accB[8 * h2 + 6], accB[8 * h2 + 7])};
    dst[(17 * 2 + h2) * 64] = pk;
  }
  STAMP(2, 5);
}

__global__ __launch_bounds__(256) void k4_reduce(const u32x4* __restrict__ part, const u16* __restrict__ h,
                                                 const u16* __restrict__ b2, u16* __restrict__ out) {
  const int gid = blockIdx.x * 256 + threadIdx.x;
  const int lane = gid & 63, rest = gid >> 6;
  const int h2 = rest & 1, dtl = (rest >> 1) & 1, r1 = rest >> 2;
  const int dp = r1 % 9, r3 = r1 / 9;
  const int t32 = lane & 31, hh = lane >> 5;
  const int tok = r3 * 32 + t32;
  const int d0 = 64 * dp + 32 * dtl + 16 * h2 + 4 * hh;
  u32x4 v[16];
#pragma unroll
  for (int c = 0; c < 16; ++c) v[c] = part[(size_t)c * 147456 + gid];
  const u16* hp = h + ((size_t)((r3 * 36 + (d0 >> 4)) * 64 + t32) * 8 + 4 * hh);
  const u32x2 ha = *(const u32x2*)hp, hb = *(const u32x2*)(hp + 256);
  const u32x2 ba = *(const u32x2*)(b2 + d0), bb = *(const u32x2*)(b2 + d0 + 8);
  float s[8];
  s[0] = bf_lo(ha[0]) + bf_lo(ba[0]); s[1] = bf_hi(ha[0]) + bf_hi(ba[0]);
  s[2] = bf_lo(ha[1]) + bf_lo(ba[1]); s[3] = bf_hi(ha[1]) + bf_hi(ba[1]);
  s[4] = bf_lo(hb[0]) + bf_lo(bb[0]); s[5] = bf_hi(hb[0]) + bf_hi(bb[0]);
  s[6] = bf_lo(hb[1]) + bf_lo(bb[1]); s[7] = bf_hi(hb[1]) + bf_hi(bb[1]);
  float a[8];
#pragma unroll
  for (int e = 0; e < 8; ++e) a[e] = 0.f;
#pragma unroll
  for (int c = 0; c < 16; ++c)
#pragma unroll
    for (int e = 0; e < 4; ++e) { a[2 * e] += bf_lo(v[c][e]); a[2 * e + 1] += bf_hi(v[c][e]); }
  u32x2 oa = {pack_bf16(s[0] + a[0], s[1] + a[1]), pack_bf16(s[2] + a[2], s[3] + a[3])};
  u32x2 ob = {pack_bf16(s[4] + a[4], s[5] + a[5]), pack_bf16(s[6] + a[6], s[7] + a[7])};
  *(u32x2*)(out + (size_t)tok * D_ + d0) = oa;
  *(u32x2*)(out + (size_t)tok * D_ + d0 + 8) = ob;
}

extern "C" void kernel_launch(void* const* d_in, const int* in_sizes, int n_in, void* d_out, int out_size,
                              void* d_ws, size_t ws_size, hipStream_t stream) {
  const u16* x = (const u16*)d_in[0];
  const u16* wqkv = (const u16*)d_in[1];
  const u16* wo = (const u16*)d_in[2];
  const u16* w1 = (const u16*)d_in[4];
  const u16* b1 = (const u16*)d_in[5];
  const u16* w2 = (const u16*)d_in[6];
  const u16* b2 = (const u16*)d_in[7];
  u16* out = (u16*)d_out;
  char* ws = (char*)d_ws;
  u16* o = (u16*)ws;
  u16* h = (u16*)(ws + 2359296);
  float* stats = (float*)(ws + 2 * 2359296);
  float* c1 = (float*)(ws + 2 * 2359296 + 196608);
  u32x4* part = (u32x4*)(ws + 2 * 2359296 + 196608 + 18432 + 2048);

  hipFuncSetAttribute((const void*)k1_qkv_attn, hipFuncAttributeMaxDynamicSharedMemorySize, K1_LDS);
  hipFuncSetAttribute((const void*)k2_proj, hipFuncAttributeMaxDynamicSharedMemorySize, K2_LDS);
  hipFuncSetAttribute((const void*)k3_mlp, hipFuncAttributeMaxDynamicSharedMemorySize, K3_LDS);

  k1_qkv_attn<<<256, 256, K1_LDS, stream>>>(x, wqkv, o);
  k2_proj<<<256, 256, K2_LDS, stream>>>(o, x, wo, w1, h, stats, c1);
  k3_mlp<<<256, 512, K3_LDS, stream>>>((const u32x4*)h, stats, c1, w1, b1, w2, part);
  k4_reduce<<<576, 256, 0, stream>>>(part, h, b2, out);
}
```

```cpp
#include <hip/hip_runtime.h>
#include <hip/hip_bf16.h>
#include <stdint.h>
#include <type_traits>

typedef short s16x4 __attribute__((ext_vector_type(4)));
typedef short s16x8 __attribute__((ext_vector_type(8)));
typedef __bf16 bf2_t __attribute__((ext_vector_type(2)));
typedef float f32x4 __attribute__((ext_vector_type(4)));
typedef float f32x16 __attribute__((ext_vector_type(16)));
typedef unsigned u32x4 __attribute__((ext_vector_type(4)));
typedef unsigned u32x2 __attribute__((ext_vector_type(2)));
typedef unsigned short u16;
typedef __attribute__((address_space(3))) const char* lds_cptr;
typedef __attribute__((address_space(3))) char* lds_ptr;
typedef __attribute__((address_space(3))) s16x4* lds_s16x4_ptr;

#define DEV __device__ __forceinline__
#define MFMA32(a, b, c) __builtin_amdgcn_mfma_f32_32x32x16_bf16(a, b, c, 0, 0, 0)
#define MFMA16(a, b, c) __builtin_amdgcn_mfma_f32_16x16x32_bf16(a, b, c, 0, 0, 0)

constexpr int S_ = 2048, D_ = 576, F_ = 4608;
constexpr float EPS = 1e-5f;

DEV s16x4 vtr(lds_cptr p) { return __builtin_amdgcn_ds_read_tr16_b64_v4i16((lds_s16x4_ptr)p); }
DEV s16x8 cat8(s16x4 lo, s16x4 hi) { return __builtin_shufflevector(lo, hi, 0, 1, 2, 3, 4, 5, 6, 7); }
DEV unsigned pack_bf16(float a, float b) {
  bf2_t v; v[0] = (__bf16)a; v[1] = (__bf16)b; return __builtin_bit_cast(unsigned, v);
}
DEV float bf_lo(unsigned u) { return __builtin_bit_cast(float, u << 16); }
DEV float bf_hi(unsigned u) { return __builtin_bit_cast(float, u & 0xffff0000u); }
DEV float dot2bf(unsigned a, unsigned b, float c) {
  return __builtin_amdgcn_fdot2_f32_bf16(__builtin_bit_cast(bf2_t, a), __builtin_bit_cast(bf2_t, b), c, false);
}
DEV void glds16(unsigned voff, const void* sbase_, unsigned ldsaddr_) {
  unsigned sv;
  const uint64_t pv = (uint64_t)sbase_;
  const uint64_t sbase = ((uint64_t)(unsigned)__builtin_amdgcn_readfirstlane((int)(pv >> 32)) << 32) |
                         (uint64_t)(unsigned)__builtin_amdgcn_readfirstlane((int)(unsigned)pv);
  const unsigned ldsaddr = (unsigned)__builtin_amdgcn_readfirstlane((int)ldsaddr_);
  asm volatile("s_mov_b32 %0, m0\n\ts_mov_b32 m0, %3\n\ts_nop 0\n\tglobal_load_lds_dwordx4 %1, %2\n\ts_mov_b32 m0, %0"
               : "=&s"(sv) : "v"(voff), "s"(sbase), "s"(ldsaddr) : "memory");
}
DEV void gtouch(unsigned voff, const void* sbase_, unsigned ldsaddr_) {
  unsigned sv;
  const uint64_t pv = (uint64_t)sbase_;
  const uint64_t sbase = ((uint64_t)(unsigned)__builtin_amdgcn_readfirstlane((int)(pv >> 32)) << 32) |
                         (uint64_t)(unsigned)__builtin_amdgcn_readfirstlane((int)(unsigned)pv);
  const unsigned ldsaddr = (unsigned)__builtin_amdgcn_readfirstlane((int)ldsaddr_);
  asm volatile("s_mov_b32 %0, m0\n\ts_mov_b32 m0, %3\n\ts_nop 0\n\tglobal_load_lds_dword %1, %2\n\ts_mov_b32 m0, %0"
               : "=&s"(sv) : "v"(voff), "s"(sbase), "s"(ldsaddr) : "memory");
}
#define WAIT_BAR(N) asm volatile("s_waitcnt vmcnt(" #N ") lgkmcnt(0)\n\ts_barrier" ::: "memory")

template <int VM, int LGKM = 15> DEV void waitcnt() {
  __builtin_amdgcn_s_waitcnt((VM & 15) | ((VM >> 4) << 14) | (7 << 4) | ((LGKM & 15) << 8));
}
DEV void barrier() { asm volatile("s_barrier" ::: "memory"); }
template <int N> DEV void wait_bar() { asm volatile("s_waitcnt vmcnt(%0) lgkmcnt(0)\n\ts_barrier" ::"i"(N) : "memory"); }
template <int B, int E, class F> DEV void static_for(F&& f) {
  if constexpr (B < E) { f(std::integral_constant<int, B>{}); static_for<B + 1, E>(f); }
}
#define AINL __attribute__((always_inline))

#define FS(i) do { } while (0)
#define STAMP(k, i) do { } while (0)
DEV float gelu_tanh(float x) {
  const float c1 = -2.0f * 1.4426950408889634f * 0.7978845608028654f;
  const float c3 = c1 * 0.044715f;
  float x2 = x * x;
  float t = x * __builtin_fmaf(x2, c3, c1);
  float e = __builtin_amdgcn_exp2f(t);
  return x * __builtin_amdgcn_rcpf(1.0f + e);
}

constexpr int K1_ROW = 288;
constexpr int K1_XO = 64 * K1_ROW;
constexpr int K1_SLOT = K1_XO + 144 * 128;
constexpr int K1_LDS = 4 * K1_SLOT;
constexpr int K1_QS = 232;

__global__ __launch_bounds__(256, 2) void k1_qkv_attn(const u16* __restrict__ x, const u16* __restrict__ wqkv,
                                                      u16* __restrict__ o) {
  extern __shared__ __attribute__((aligned(16))) char smem[];
  const int tid = threadIdx.x, lane = tid & 63;
  const int wid = __builtin_amdgcn_readfirstlane(tid >> 6);
  const int b = blockIdx.x, xcd = b & 7, bi = b >> 3;
  const int hp = 2 * xcd + (bi & 1), pnl = bi >> 1;
  const int par = hp & 1;
  const int i16 = lane & 15, g = lane >> 4, q4 = i16 >> 2, p4 = i16 & 3;
  const unsigned lds0 = (unsigned)(uintptr_t)smem;
  const int T0 = pnl * 128 + wid * 32;
  STAMP(0, 0);

  unsigned wo_[5], xo_[5];
  {
    const int segbase = (72 * hp) & ~15;
#pragma unroll
    for (int j = 0; j < 5; ++j) {
      const int n = j < 4 ? wid + 4 * j : 16 + (wid & 1);
      const int idx = n * 64 + lane;
      const int prow = idx / 18; int ch = idx - prow * 18; ch = ch > 14 ? 14 : ch;
      const int q = (prow >> 2) & 3;
      const int row = (prow & ~15) | ((q == 1 ? 2 : (q == 2 ? 1 : q)) << 2) | (prow & 3);
      const int seg = ch / 5, cc = ch - seg * 5;
      wo_[j] = (unsigned)(row * 3456 + seg * 1152 + segbase + cc * 16);
      const int xrow = idx >> 3, xch = idx & 7;
      int tok = pnl * 128 - 16 + xrow; tok = tok < 0 ? 0 : tok;
      xo_[j] = (unsigned)(tok * 1152 + ((xch ^ (xrow & 7)) * 16));
    }
  }
  auto issue_stage = [&](int st) AINL {
    const unsigned dst = lds0 + (unsigned)((st & 3) * K1_SLOT);
    const char* wb = (const char*)wqkv + (size_t)st * 64 * 3456;
    const char* xbp = (const char*)x + st * 128;
#pragma unroll
    for (int j = 0; j < 4; ++j) {
      glds16(wo_[j], wb, dst + (unsigned)((wid + 4 * j) * 1024));
      glds16(xo_[j], xbp, dst + (unsigned)(K1_XO + (wid + 4 * j) * 1024));
    }
    if (wid < 2) glds16(wo_[4], wb, dst + (unsigned)((16 + wid) * 1024));
    else glds16(xo_[4], xbp, dst + (unsigned)(K1_XO + (16 + wid - 2) * 1024));
  };
  issue_stage(0); issue_stage(1); issue_stage(2); issue_stage(3);
  STAMP(0, 1);

  f32x4 acc[7][3];
#pragma unroll
  for (int ft = 0; ft < 7; ++ft)
#pragma unroll
    for (int ct = 0; ct < 3; ++ct) acc[ft][ct] = f32x4{0.f, 0.f, 0.f, 0.f};
  int aoff[7];
#pragma unroll
  for (int ft = 0; ft < 7; ++ft) {
    int G = 4 * ft + p4; G = G > 26 ? 26 : G;
    const int seg = G / 9, wi = G - seg * 9;
    aoff[ft] = (16 * (g >> 1) + 4 * (g & 1) + q4) * K1_ROW + seg * 80 + par * 8 + wi * 8;
  }
  int boff[3];
#pragma unroll
  for (int ct = 0; ct < 3; ++ct) {
    const int row = wid * 32 + ct * 16 + i16;
    boff[ct] = K1_XO + row * 128 + ((g ^ (row & 7)) * 16);
  }
  f32x4 asum[3], asq[3];
#pragma unroll
  for (int ct = 0; ct < 3; ++ct) { asum[ct] = f32x4{0.f, 0.f, 0.f, 0.f}; asq[ct] = f32x4{0.f, 0.f, 0.f, 0.f}; }
  const s16x8 ones8 = {0x3f80, 0x3f80, 0x3f80, 0x3f80, 0x3f80, 0x3f80, 0x3f80, 0x3f80};
  s16x8 xbA[3], xbB[3];
  s16x4 afA[14], afB[14];
  auto load_job = [&](auto J_, auto KS_, int st, s16x8 (&xb)[3], s16x4 (&af)[14]) AINL {
    constexpr int j = decltype(J_)::value, ks = decltype(KS_)::value;
    lds_cptr sl = (lds_cptr)smem + (st & 3) * K1_SLOT;
    if constexpr (j < 3) xb[j] = *(__attribute__((address_space(3))) const s16x8*)(sl + (boff[j] ^ (ks * 64)));
    else af[j - 3] = vtr(sl + aoff[(j - 3) >> 1] + ks * 32 * K1_ROW + ((j - 3) & 1) * 8 * K1_ROW);
  };
  auto issue_piece = [&](int st, auto J_) AINL {
    constexpr int j = decltype(J_)::value;
    const unsigned dst = lds0 + (unsigned)((st & 3) * K1_SLOT);
    const char* wb = (const char*)wqkv + (size_t)st * 64 * 3456;
    const char* xbp = (const char*)x + st * 128;
    if constexpr (j < 4) glds16(wo_[j], wb, dst + (unsigned)((wid + 4 * j) * 1024));
    else if constexpr (j < 8) glds16(xo_[j - 4], xbp, dst + (unsigned)(K1_XO + (wid + 4 * (j - 4)) * 1024));
    else { if (wid < 2) glds16(wo_[4], wb, dst + (unsigned)((16 + wid) * 1024)); else glds16(xo_[4], xbp, dst + (unsigned)(K1_XO + (16 + wid - 2) * 1024)); }
  };
  auto half = [&](auto KS_, int st, s16x8 (&xc)[3], s16x4 (&ac)[14], s16x8 (&xn)[3], s16x4 (&an)[14]) AINL {
    constexpr int ks = decltype(KS_)::value;
    if constexpr (ks == 1) {
      FS(3 * st + 1);
      if (st < 6) waitcnt<18, 0>(); else if (st == 6) waitcnt<9, 0>(); else waitcnt<0, 0>();
      barrier();
      FS(3 * st + 2);
    } else { FS(3 * st); }
    s16x8 xh;
    static_for<0, 27>([&](auto M_) AINL {
      constexpr int r = M_;
      if constexpr (r < 3) asq[r] = MFMA16(xc[r], xc[r], asq[r]);
      else if constexpr (r < 6) asum[r - 3] = MFMA16(ones8, xc[r - 3], asum[r - 3]);
      else {
        constexpr int ft = (r - 6) / 3, ct = (r - 6) % 3;
        acc[ft][ct] = MFMA16(cat8(ac[2 * ft], ac[2 * ft + 1]), ct == 0 ? xh : xc[ct], acc[ft][ct]);
      }
      if constexpr (r == 4) { xh = xc[0]; if (i16 == 0) xh = ones8; }
      {
        constexpr int u = r - (r + 1) / 5;
        if constexpr (r % 5 != 3 && u < 17) load_job(std::integral_constant<int, u>{}, std::integral_constant<int, 1 - ks>{}, st + ks, xn, an);
      }
      if constexpr (ks == 1 && r % 5 == 3) { if (st + 4 < 9) issue_piece(st + 4, std::integral_constant<int, r / 5>{}); }
      if constexpr (ks == 0 && r % 5 == 3 && r / 5 < 4) { if (st >= 1 && st + 3 < 9) issue_piece(st + 3, std::integral_constant<int, 5 + r / 5>{}); }
      __builtin_amdgcn_sched_barrier(0);
    });
  };
  using KS0 = std::integral_constant<int, 0>; using KS1 = std::integral_constant<int, 1>;
  waitcnt<27>(); barrier();
  STAMP(0, 6);
  static_for<0, 17>([&](auto J_) AINL { load_job(J_, KS0{}, 0, xbA, afA); });
#pragma unroll 1
  for (int st = 0; st < 9; ++st) {
    half(KS0{}, st, xbA, afA, xbB, afB);
    half(KS1{}, st, xbB, afB, xbA, afA);
  }
  float ssum[3], ssq[3];
#pragma unroll
  for (int ct = 0; ct < 3; ++ct) {
    ssum[ct] = asum[ct][0];
    float dsel = (i16 & 2) ? ((i16 & 1) ? asq[ct][3] : asq[ct][2]) : ((i16 & 1) ? asq[ct][1] : asq[ct][0]);
    ssq[ct] = __shfl(dsel, (i16 >> 2) * 16 + i16);
  }
  STAMP(0, 2);
  WAIT_BAR(0);
  lds_ptr qs = (lds_ptr)smem + wid * (48 * K1_QS);
#pragma unroll
  for (int ct = 0; ct < 3; ++ct) {
    const float mean = ssum[ct] * (1.0f / 576.0f);
    float var = ssq[ct] * (1.0f / 576.0f) - mean * mean;
    var = var < 0.f ? 0.f : var;
    const float rs = 1.0f / sqrtf(var + EPS);
    const float sm = rs * mean;
#pragma unroll
    for (int ft = 0; ft < 7; ++ft) {
      const int G = 4 * ft + g;
      float v[4];
#pragma unroll
      for (int r = 0; r < 4; ++r) {
        const float c = __shfl(acc[ft][0][r], lane & 48);
        v[r] = rs * acc[ft][ct][r] - sm * c;
      }
      if (G < 27) {
        u32x2 pk = {pack_bf16(v[0], v[1]), pack_bf16(v[2], v[3])};
        *(__attribute__((address_space(3))) u32x2*)(qs + (ct * 16 + i16) * K1_QS + G * 8) = pk;
      }
    }
  }
  asm volatile("s_waitcnt lgkmcnt(0)" ::: "memory");
  STAMP(0, 3);
  {
    const int tk = lane & 31, hd = lane >> 5;
    lds_cptr qrow = (lds_cptr)qs + (16 + tk) * K1_QS + hd * 36;
    unsigned qv[9];
#pragma unroll
    for (int d = 0; d < 9; ++d) qv[d] = *(__attribute__((address_space(3))) const unsigned*)(qrow + d * 4);
    float lg[8];
#pragma unroll
    for (int w = 0; w < 8; ++w) {
      lds_cptr krow = qrow - 2 * w * K1_QS + 72;
      float a = 0.f;
#pragma unroll
      for (int d = 0; d < 9; ++d) a = dot2bf(qv[d], *(__attribute__((address_space(3))) const unsigned*)(krow + d * 4), a);
      lg[w] = (T0 + tk - 2 * w >= 0) ? a * 0.23570226039551584f : -1e9f;
    }
    float mx = lg[0];
#pragma unroll
    for (int w = 1; w < 8; ++w) mx = fmaxf(mx, lg[w]);
    float se = 0.f;
#pragma unroll
    for (int w = 0; w < 8; ++w) { lg[w] = __builtin_amdgcn_exp2f((lg[w] - mx) * 1.4426950408889634f); se += lg[w]; }
    const float inv = 1.0f / se;
    float ov[18];
#pragma unroll
    for (int d = 0; d < 18; ++d) ov[d] = 0.f;
#pragma unroll
    for (int w = 0; w < 8; ++w) {
      lds_cptr vrow = qrow - 2 * w * K1_QS + 144;
      const float pw = lg[w] * inv;
#pragma unroll
      for (int d = 0; d < 9; ++d) {
        const unsigned u = *(__attribute__((address_space(3))) const unsigned*)(vrow + d * 4);
        ov[2 * d] = __builtin_fmaf(pw, bf_lo(u), ov[2 * d]);
        ov[2 * d + 1] = __builtin_fmaf(pw, bf_hi(u), ov[2 * d + 1]);
      }
    }
    {
      const int tok = T0 + tk, c0 = hp * 36 + hd * 18;
      char* ob_ = (char*)o + (size_t)((tok >> 4) * 18 * 64 + (tok & 15)) * 16;
#pragma unroll
      for (int d = 0; d < 9; ++d) {
        const int col = c0 + 2 * d;
        *(unsigned*)(ob_ + (col >> 3) * 256 + (col & 7) * 2) = pack_bf16(ov[2 * d], ov[2 * d + 1]);
      }
    }
  }
  STAMP(0, 4); STAMP(0, 5);
}

constexpr int K2_LDS = 56 * 1024;

__global__ __launch_bounds__(256, 1) void k2_proj(const u16* __restrict__ o, const u16* __restrict__ x,
                                                  const u16* __restrict__ wo, const u16* __restrict__ w1,
                                                  u16* __restrict__ h, float* __restrict__ stats, float* __restrict__ c1) {
  extern __shared__ __attribute__((aligned(16))) char smem[];
  const int tid = threadIdx.x, lane = tid & 63;
  const int b = blockIdx.x;
  if (b >= 192) {
    const int cb = b - 192;
    const int cg = tid % 9, rg = tid / 9;
    float* red = (float*)smem;
    if (rg < 24) {
      u32x4 v[24];
#pragma unroll
      for (int i = 0; i < 24; ++i) v[i] = *(const u32x4*)(w1 + (size_t)(rg * 24 + i) * F_ + cb * 72 + cg * 8);
      float s[8];
#pragma unroll
      for (int e = 0; e < 8; ++e) s[e] = 0.f;
#pragma unroll
      for (int i = 0; i < 24; ++i)
#pragma unroll
        for (int e = 0; e < 4; ++e) { s[2 * e] += bf_lo(v[i][e]); s[2 * e + 1] += bf_hi(v[i][e]); }
#pragma unroll
      for (int e = 0; e < 8; ++e) red[rg * 72 + cg * 8 + e] = s[e];
    }
    __syncthreads();
    if (tid < 72) {
      float s = 0.f;
      for (int r = 0; r < 24; ++r) s += red[r * 72 + tid];
      c1[cb * 72 + tid] = s;
    }
    return;
  }
  const int wid = __builtin_amdgcn_readfirstlane(tid >> 6);
  STAMP(1, 0);
  const int xcd = b & 7, bi = b >> 3;
  const int pnl = 2 * xcd + (bi >= 12 ? 1 : 0), n = bi >= 12 ? bi - 12 : bi;
  const int i16 = lane & 15, g = lane >> 4, q4 = i16 >> 2, p4 = i16 & 3;
  const unsigned lds0 = (unsigned)(uintptr_t)smem;
  const int T0 = pnl * 128 + wid * 32;

  s16x8 ob[2][18];
#pragma unroll
  for (int ct = 0; ct < 2; ++ct) {
    const u32x4* op_ = (const u32x4*)o + (size_t)((T0 >> 4) + ct) * 18 * 64 + lane;
#pragma unroll
    for (int ks = 0; ks < 18; ++ks) ob[ct][ks] = __builtin_bit_cast(s16x8, op_[ks * 64]);
  }
#pragma unroll
  for (int j = 0; j < 14; ++j) {
    int ni = j * 4 + wid; ni = ni > 53 ? 53 : ni;
    const int idx = ni * 64 + lane;
    const int row = idx / 6, ch = idx - row * 6;
    glds16((unsigned)(row * 1152 + n * 96 + ch * 16), wo, lds0 + (unsigned)(ni * 1024));
  }
  u32x2 xr[2][3];
#pragma unroll
  for (int ct = 0; ct < 2; ++ct)
#pragma unroll
    for (int ft = 0; ft < 3; ++ft)
      xr[ct][ft] = *(const u32x2*)(x + (size_t)(T0 + ct * 16 + i16) * D_ + n * 48 + ft * 16 + 4 * g);

  f32x4 acc[3][2];
#pragma unroll
  for (int ft = 0; ft < 3; ++ft)
#pragma unroll
    for (int ct = 0; ct < 2; ++ct) acc[ft][ct] = f32x4{0.f, 0.f, 0.f, 0.f};
  lds_cptr abase = (lds_cptr)smem + (8 * g + q4) * 96 + p4 * 8;
  STAMP(1, 1);
  WAIT_BAR(0);
  STAMP(1, 2);
#pragma unroll
  for (int ks = 0; ks < 18; ++ks)
#pragma unroll
    for (int ft = 0; ft < 3; ++ft) {
      s16x8 a = cat8(vtr(abase + ks * 32 * 96 + ft * 32), vtr(abase + ks * 32 * 96 + ft * 32 + 4 * 96));
#pragma unroll
      for (int ct = 0; ct < 2; ++ct) acc[ft][ct] = MFMA16(a, ob[ct][ks], acc[ft][ct]);
    }
#pragma unroll
  for (int ct = 0; ct < 2; ++ct) {
    const int tok = T0 + ct * 16 + i16;
    float hv[12];
#pragma unroll
    for (int ft = 0; ft < 3; ++ft) {
      const unsigned p0 = pack_bf16(bf_lo(xr[ct][ft][0]) + acc[ft][ct][0], bf_hi(xr[ct][ft][0]) + acc[ft][ct][1]);
      const unsigned p1 = pack_bf16(bf_lo(xr[ct][ft][1]) + acc[ft][ct][2], bf_hi(xr[ct][ft][1]) + acc[ft][ct][3]);
      u32x2 pk = {p0, p1};
      {
        const int col = n * 48 + ft * 16 + 4 * g;
        const int ks = col >> 4, hh2 = (col >> 3) & 1, j4 = col & 4;
        *(u32x2*)(h + ((size_t)(((tok >> 5) * 36 + ks) * 64 + hh2 * 32 + (tok & 31)) * 8 + j4)) = pk;
      }
      hv[ft * 4 + 0] = bf_lo(p0); hv[ft * 4 + 1] = bf_hi(p0); hv[ft * 4 + 2] = bf_lo(p1); hv[ft * 4 + 3] = bf_hi(p1);
    }
    float s = 0.f;
#pragma unroll
    for (int e = 0; e < 12; ++e) s += hv[e];
    s += __shfl_xor(s, 16); s += __shfl_xor(s, 32);
    const float mean = s * (1.0f / 48.0f);
    float m2 = 0.f;
#pragma unroll
    for (int e = 0; e < 12; ++e) { const float d = hv[e] - mean; m2 = __builtin_fmaf(d, d, m2); }
    m2 += __shfl_xor(m2, 16); m2 += __shfl_xor(m2, 32);
    if (g == 0) { stats[(size_t)tok * 24 + n * 2] = s; stats[(size_t)tok * 24 + n * 2 + 1] = m2; }
  }
  STAMP(1, 3); STAMP(1, 4);
}

constexpr int HS = 18432;
constexpr int K3_XR = 4 * HS;
constexpr int K3_TAB = K3_XR + 73728;
constexpr int K3_SCR = K3_TAB + 288 * 8;
constexpr int K3_LDS = K3_SCR + 256;

__global__ __launch_bounds__(512, 2) void k3_mlp(const u32x4* __restrict__ hT, const float* __restrict__ stats,
                                                 const float* __restrict__ c1, const u16* __restrict__ w1,
                                                 const u16* __restrict__ b1, const u16* __restrict__ w2,
                                                 u32x4* __restrict__ part) {
  extern __shared__ __attribute__((aligned(16))) char smem[];
  const int tid = threadIdx.x, lane = tid & 63;
  const int wid = __builtin_amdgcn_readfirstlane(tid >> 6);
  const int b = blockIdx.x, xcd = b & 7, bi = b >> 3;
  const int c = 2 * xcd + (bi & 1), pnl = bi >> 1;
  const unsigned lds0 = (unsigned)(uintptr_t)smem;
  STAMP(2, 0);

  if (wid >= 4) {
    const int lw = wid - 4;
    unsigned o1[5], o2[5];
#pragma unroll
    for (int j = 0; j < 5; ++j) {
      const int n = j < 4 ? lw + 4 * j : 16 + (lw & 1);
      const int row = n * 16 + (lane >> 2), ch = lane & 3;
      o1[j] = (unsigned)(row * (F_ * 2) + ch * 16);
      o2[j] = (unsigned)(row * (D_ * 2) + ch * 16);
    }
    const char* w1c = (const char*)w1 + (size_t)(288 * c) * 2;
    const char* w2c = (const char*)w2 + (size_t)(288 * c) * D_ * 2;
    auto issue_step = [&](int s) AINL {
      const bool is2 = s >= 18;
      const char* base = is2 ? w2c + (size_t)(64 * (s - 18)) : w1c + ((size_t)(288 * (s & 1)) * F_ + 32 * (s >> 1)) * 2;
      const unsigned dst = lds0 + (unsigned)((s & 3) * HS);
#pragma unroll
      for (int j = 0; j < 4; ++j) glds16(is2 ? o2[j] : o1[j], base, dst + (unsigned)((lw + 4 * j) * 1024));
      if ((lw >> 1) == (s & 1)) glds16(is2 ? o2[4] : o1[4], base, dst + (unsigned)((16 + (lw & 1)) * 1024));
    };
    {
      const unsigned scr = lds0 + (unsigned)K3_SCR;
      const int idx = lw * 64 + lane;
      int r = idx / 6, l = idx - r * 6; r = r > 35 ? 35 : r;
      unsigned colb = (unsigned)((576 * c) & ~127) + l * 128;
      colb = colb > 9216 - 128 ? 9216 - 128 : colb;
      gtouch((unsigned)((36 * pnl + r) * 9216) + colb, w1, scr);
      int i2 = idx > 161 ? 161 : idx;
      if (lw < 3) gtouch((unsigned)((162 * pnl + i2) * 128), w2c, scr);
    }
    issue_step(0); issue_step(1); issue_step(2);
    {
      float* tc = (float*)(smem + K3_TAB);
      const int t = tid - 256;
      tc[t] = c1[288 * c + t]; tc[288 + t] = bf_lo(b1[288 * c + t]);
      if (t < 32) { tc[256 + t] = c1[288 * c + 256 + t]; tc[288 + 256 + t] = bf_lo(b1[288 * c + 256 + t]); }
    }
    waitcnt<4, 0>(); barrier();
#pragma unroll 1
    for (int s = 0; s < 36; ++s) {
      if (s + 3 < 36) issue_step(s + 3);
      if (s >= 20) {
        const int d = s - 20;
        lds_cptr sp = (lds_cptr)smem + K3_XR + lw * 18432 + (d & 1) * 2048 + lane * 16;
        const u32x4 q0 = *(__attribute__((address_space(3))) const u32x4*)sp;
        const u32x4 q1 = *(__attribute__((address_space(3))) const u32x4*)(sp + 1024);
        u32x4* dq = part + (size_t)((((c * 16 + pnl) * 4 + lw) * 36) + 2 * d) * 64 + lane;
        dq[0] = q0; dq[64] = q1;
        if (s + 3 < 36) waitcnt<6>(); else waitcnt<2>();
      } else {
        if (s + 3 < 36) waitcnt<4>(); else waitcnt<0>();
      }
      barrier();
    }
    {
      lds_cptr sp = (lds_cptr)smem + K3_XR + lw * 18432 + lane * 16;
      const u32x4 q0 = *(__attribute__((address_space(3))) const u32x4*)sp;
      const u32x4 q1 = *(__attribute__((address_space(3))) const u32x4*)(sp + 1024);
      u32x4* dq = part + (size_t)((((c * 16 + pnl) * 4 + lw) * 36) + 2 * 16) * 64 + lane;
      dq[0] = q0; dq[64] = q1;
    }
    return;
  }

  const int t32 = lane & 31, hh = lane >> 5;
  const int tok = pnl * 128 + wid * 32 + t32;
  const int i16 = lane & 15, q4 = i16 >> 2, p4 = i16 & 3, blk = (lane >> 4) & 1;
  s16x8 hb[36];
  {
    const u32x4* hp = hT + (size_t)((pnl * 4 + wid) * 36) * 64 + lane;
#pragma unroll
    for (int ks = 0; ks < 36; ++ks) hb[ks] = __builtin_bit_cast(s16x8, hp[ks * 64]);
  }
  float sc, scm;
  {
    f32x4 stv[6];
#pragma unroll
    for (int i = 0; i < 6; ++i) stv[i] = *(const f32x4*)(stats + (size_t)tok * 24 + i * 4);
    float sum = 0.f;
#pragma unroll
    for (int i = 0; i < 6; ++i) sum += stv[i][0] + stv[i][2];
    const float mean = sum * (1.0f / 576.0f);
    float m2 = 0.f;
#pragma unroll
    for (int i = 0; i < 6; ++i) {
      const float d0 = stv[i][0] * (1.0f / 48.0f) - mean, d1 = stv[i][2] * (1.0f / 48.0f) - mean;
      m2 += stv[i][1] + stv[i][3] + 48.0f * (d0 * d0 + d1 * d1);
    }
    const float var = m2 * (1.0f / 576.0f);
    const float r1 = __builtin_amdgcn_rsqf(var + EPS);
    const float var2 = var * r1 * r1;
    const float r2 = __builtin_amdgcn_rsqf(var2 + EPS);
    sc = r1 * r2; scm = sc * mean;
  }
  const int lch = (16 * blk + 4 * p4) * 2;
  lds_cptr lb1 = (lds_cptr)smem + (8 * hh + q4) * 64 + lch;
  lds_cptr lb2 = (lds_cptr)smem + (4 * hh + q4) * 64 + lch;
  lds_ptr xw = (lds_ptr)smem + K3_XR + wid * 18432 + lane * 16;

  constexpr int LA = 4;
  s16x8 fr[LA];
  f32x16 accA, accB, zero16;
#pragma unroll
  for (int r = 0; r < 16; ++r) { accA[r] = 0.f; accB[r] = 0.f; zero16[r] = 0.f; }
  using TT = std::true_type; using FF = std::false_type;
  using P0 = std::integral_constant<int, 0>; using P2 = std::integral_constant<int, 2>;

  STAMP(2, 1);
  waitcnt<0, 0>(); barrier();
  STAMP(2, 2);
  static_for<0, LA>([&](auto M) AINL {
    constexpr int m = M;
    lds_cptr a = lb1 + m * 16 * 64;
    fr[m] = cat8(vtr(a), vtr(a + 4 * 64));
  });

  auto pass1 = [&](auto LAST_, f32x16& acc, const f32x16& accp, int it) AINL {
    constexpr bool LAST = decltype(LAST_)::value;
    const int ftp = it == 0 ? 0 : it - 1;
    const int oA = ((2 * it) & 3) * HS, oN = ((2 * it + 2) & 3) * HS;
    unsigned gp[8];
    float v0 = 0.f, v1 = 0.f, u0 = 0.f, u1 = 0.f;
    f32x4 tcc, tbb;
    static_for<0, 36>([&](auto MM) AINL {
      constexpr int m = MM;
      if constexpr (m == 18) barrier();
      acc = MFMA32(fr[m % LA], hb[m], m == 0 ? zero16 : acc);
      constexpr int mn = m + LA;
      lds_cptr a = mn < 36 ? lb1 + oA + (mn / 18) * HS + (mn % 18) * 16 * 64 : (LAST ? lb2 : lb1) + oN + (mn - 36) * 16 * 64;
      constexpr int r2 = (mn >= 36 && LAST) ? 8 * 64 : 4 * 64;
      s16x4 flo, fhi;
      constexpr int ph = (m - 4) & 3, e = ((m - 4) >> 2) * 2;
      const float gc1 = -2.0f * 1.4426950408889634f * 0.7978845608028654f, gc3 = gc1 * 0.044715f;
      if constexpr (m >= 4 && ph == 0) {
        u0 = __builtin_fmaf(-scm, tcc[e & 3], tbb[e & 3]); u1 = __builtin_fmaf(-scm, tcc[(e & 3) + 1], tbb[(e & 3) + 1]);
        flo = vtr(a);
        v0 = __builtin_fmaf(sc, accp[e], u0); v1 = __builtin_fmaf(sc, accp[e + 1], u1);
        fhi = vtr(a + r2);
        u0 = v0 * v0;
      } else if constexpr (m >= 4 && ph == 1) {
        u1 = v1 * v1; u0 = __builtin_fmaf(u0, gc3, gc1);
        flo = vtr(a);
        u1 = __builtin_fmaf(u1, gc3, gc1); u0 = v0 * u0;
        fhi = vtr(a + r2);
        u1 = v1 * u1;
      } else if constexpr (m >= 4 && ph == 2) {
        u0 = __builtin_amdgcn_exp2f(u0); u1 = __builtin_amdgcn_exp2f(u1);
        flo = vtr(a); fhi = vtr(a + r2);
        u0 = 1.0f + u0; u1 = 1.0f + u1;
      } else if constexpr (m >= 4 && ph == 3) {
        u0 = __builtin_amdgcn_rcpf(u0); u1 = __builtin_amdgcn_rcpf(u1);
        flo = vtr(a); fhi = vtr(a + r2);
        gp[e >> 1] = pack_bf16(v0 * u0, v1 * u1);
        asm volatile("" : "+v"(gp[e >> 1]));
      } else { flo = vtr(a); fhi = vtr(a + r2); }
      asm volatile("" : "+v"(v0), "+v"(v1), "+v"(u0), "+v"(u1));
      if constexpr ((m & 7) == 1 && m < 32) {
        lds_cptr tc = (lds_cptr)smem + K3_TAB + (32 * ftp + 4 * hh + m - 1) * 4;
        tcc = *(__attribute__((address_space(3))) const f32x4*)tc;
        tbb = *(__attribute__((address_space(3))) const f32x4*)(tc + 288 * 4);
      }
      if constexpr (m == 20) *(__attribute__((address_space(3))) u32x4*)(xw + (ftp * 2) * 1024) = u32x4{gp[0], gp[1], gp[2], gp[3]};
      fr[m % LA] = cat8(flo, fhi);
      __builtin_amdgcn_sched_barrier(0);
    });
    *(__attribute__((address_space(3))) u32x4*)(xw + (ftp * 2 + 1) * 1024) = u32x4{gp[4], gp[5], gp[6], gp[7]};
    barrier();
  };
#pragma unroll 1
  for (int i2 = 0; i2 < 4; ++i2) {
    pass1(FF{}, accA, accB, 2 * i2);
    pass1(FF{}, accB, accA, 2 * i2 + 1);
  }
  pass1(TT{}, accA, accB, 8);
  STAMP(2, 3);
  {
    float gv[16];
#pragma unroll
    for (int rg = 0; rg < 4; ++rg) {
      lds_cptr tc = (lds_cptr)smem + K3_TAB + (32 * 8 + 4 * hh + 8 * rg) * 4;
      const f32x4 cc = *(__attribute__((address_space(3))) const f32x4*)tc;
      const f32x4 bb = *(__attribute__((address_space(3))) const f32x4*)(tc + 288 * 4);
#pragma unroll
      for (int e = 0; e < 4; ++e) gv[rg * 4 + e] = gelu_tanh(__builtin_fmaf(sc, accA[rg * 4 + e], __builtin_fmaf(-scm, cc[e], bb[e])));
    }
#pragma unroll
    for (int s = 0; s < 2; ++s) {
      u32x4 pk = {pack_bf16(gv[8 * s], gv[8 * s + 1]), pack_bf16(gv[8 * s + 2], gv[8 * s + 3]),
                  pack_bf16(gv[8 * s + 4], gv[8 * s + 5]), pack_bf16(gv[8 * s + 6], gv[8 * s + 7])};
      *(__attribute__((address_space(3))) u32x4*)(xw + (16 + s) * 1024) = pk;
    }
  }
  s16x8 xbf[18];
#pragma unroll
  for (int k = 0; k < 18; ++k) xbf[k] = *(__attribute__((address_space(3))) const s16x8*)(xw + k * 1024);
  STAMP(2, 4);

  u32x4* dst = part + (size_t)(((c * 16 + pnl) * 4 + wid) * 36) * 64 + lane;
  auto pass2 = [&](auto LAST_, auto PH_, f32x16& acc, const f32x16& accp, int d) AINL {
    constexpr bool LAST = decltype(LAST_)::value;
    constexpr int PH = decltype(PH_)::value;
    const int oA = ((18 + d) & 3) * HS, oN = ((19 + d) & 3) * HS;
    lds_ptr sw = xw + ((d + 1) & 1) * 2048;
    u32x4 pk0, pk1;
    static_for<0, 18>([&](auto MM) AINL {
      constexpr int m = MM;
      acc = MFMA32(fr[(m + PH) % LA], xbf[m], m == 0 ? zero16 : acc);
      constexpr int mn = m + LA;
      if constexpr (mn < 18) { lds_cptr a = lb2 + oA + mn * 16 * 64; fr[(m + PH) % LA] = cat8(vtr(a), vtr(a + 8 * 64)); }
      else if constexpr (!LAST) { lds_cptr a = lb2 + oN + (mn - 18) * 16 * 64; fr[(m + PH) % LA] = cat8(vtr(a), vtr(a + 8 * 64)); }
      if constexpr (m >= 2 && m < 6) { pk0[m - 2] = pack_bf16(accp[2 * (m - 2)], accp[2 * (m - 2) + 1]); pk1[m - 2] = pack_bf16(accp[8 + 2 * (m - 2)], accp[8 + 2 * (m - 2) + 1]); }
      if constexpr (m == 6) *(__attribute__((address_space(3))) u32x4*)sw = pk0;
      if constexpr (m == 7) *(__attribute__((address_space(3))) u32x4*)(sw + 1024) = pk1;
      __builtin_amdgcn_sched_barrier(0);
    });
    if constexpr (!LAST) barrier();
  };
#pragma unroll 1
  for (int d2 = 0; d2 < 8; ++d2) {
    pass2(FF{}, P0{}, accA, accB, 2 * d2);
    pass2(FF{}, P2{}, accB, accA, 2 * d2 + 1);
  }
  pass2(FF{}, P0{}, accA, accB, 16);
  pass2(TT{}, P2{}, accB, accA, 17);
  barrier();
#pragma unroll
  for (int h2 = 0; h2 < 2; ++h2) {
    u32x4 pk = {pack_bf16(accB[8 * h2], accB[8 * h2 + 1]), pack_bf16(accB[8 * h2 + 2], accB[8 * h2 + 3]),
                pack_bf16(accB[8 * h2 + 4], accB[8 * h2 + 5]), pack_bf16(accB[8 * h2 + 6], accB[8 * h2 + 7])};
    dst[(17 * 2 + h2) * 64] = pk;
  }
  STAMP(2, 5);
}

__global__ __launch_bounds__(256) void k4_reduce(const u32x4* __restrict__ part, const u16* __restrict__ h,
                                                 const u16* __restrict__ b2, u16* __restrict__ out) {
  const int gid = blockIdx.x * 256 + threadIdx.x;
  const int lane = gid & 63, rest = gid >> 6;
  const int h2 = rest & 1, dtl = (rest >> 1) & 1, r1 = rest >> 2;
  const int dp = r1 % 9, r3 = r1 / 9;
  const int t32 = lane & 31, hh = lane >> 5;
  const int tok = r3 * 32 + t32;
  const int d0 = 64 * dp + 32 * dtl + 16 * h2 + 4 * hh;
  u32x4 v[16];
#pragma unroll
  for (int c = 0; c < 16; ++c) v[c] = part[(size_t)c * 147456 + gid];
  const u16* hp = h + ((size_t)((r3 * 36 + (d0 >> 4)) * 64 + t32) * 8 + 4 * hh);
  const u32x2 ha = *(const u32x2*)hp, hb = *(const u32x2*)(hp + 256);
  const u32x2 ba = *(const u32x2*)(b2 + d0), bb = *(const u32x2*)(b2 + d0 + 8);
  float s[8];
  s[0] = bf_lo(ha[0]) + bf_lo(ba[0]); s[1] = bf_hi(ha[0]) + bf_hi(ba[0]);
  s[2] = bf_lo(ha[1]) + bf_lo(ba[1]); s[3] = bf_hi(ha[1]) + bf_hi(ba[1]);
  s[4] = bf_lo(hb[0]) + bf_lo(bb[0]); s[5] = bf_hi(hb[0]) + bf_hi(bb[0]);
  s[6] = bf_lo(hb[1]) + bf_lo(bb[1]); s[7] = bf_hi(hb[1]) + bf_hi(bb[1]);
  float a[8];
#pragma unroll
  for (int e = 0; e < 8; ++e) a[e] = 0.f;
#pragma unroll
  for (int c = 0; c < 16; ++c)
#pragma unroll
    for (int e = 0; e < 4; ++e) { a[2 * e] += bf_lo(v[c][e]); a[2 * e + 1] += bf_hi(v[c][e]); }
  u32x2 oa = {pack_bf16(s[0] + a[0], s[1] + a[1]), pack_bf16(s[2] + a[2], s[3] + a[3])};
  u32x2 ob = {pack_bf16(s[4] + a[4], s[5] + a[5]), pack_bf16(s[6] + a[6], s[7] + a[7])};
  *(u32x2*)(out + (size_t)tok * D_ + d0) = oa;
  *(u32x2*)(out + (size_t)tok * D_ + d0 + 8) = ob;
}

extern "C" void kernel_launch(void* const* d_in, const int* in_sizes, int n_in, void* d_out, int out_size,
                              void* d_ws, size_t ws_size, hipStream_t stream) {
  const u16* x = (const u16*)d_in[0];
  const u16* wqkv = (const u16*)d_in[1];
  const u16* wo = (const u16*)d_in[2];
  const u16* w1 = (const u16*)d_in[4];
  const u16* b1 = (const u16*)d_in[5];
  const u16* w2 = (const u16*)d_in[6];
  const u16* b2 = (const u16*)d_in[7];
  u16* out = (u16*)d_out;
  char* ws = (char*)d_ws;
  u16* o = (u16*)ws;
  u16* h = (u16*)(ws + 2359296);
  float* stats = (float*)(ws + 2 * 2359296);
  float* c1 = (float*)(ws + 2 * 2359296 + 196608);
  u32x4* part = (u32x4*)(ws + 2 * 2359296 + 196608 + 18432 + 2048);

  hipFuncSetAttribute((const void*)k1_qkv_attn, hipFuncAttributeMaxDynamicSharedMemorySize, K1_LDS);
  hipFuncSetAttribute((const void*)k2_proj, hipFuncAttributeMaxDynamicSharedMemorySize, K2_LDS);
  hipFuncSetAttribute((const void*)k3_mlp, hipFuncAttributeMaxDynamicSharedMemorySize, K3_LDS);

  k1_qkv_attn<<<256, 256, K1_LDS, stream>>>(x, wqkv, o);
  k2_proj<<<256, 256, K2_LDS, stream>>>(o, x, wo, w1, h, stats, c1);
  k3_mlp<<<256, 512, K3_LDS, stream>>>((const u32x4*)h, stats, c1, w1, b1, w2, part);
  k4_reduce<<<576, 256, 0, stream>>>(part, h, b2, out);
}
```

```cpp
#include <hip/hip_runtime.h>
#include <hip/hip_bf16.h>
#include <stdint.h>
#include <type_traits>

typedef short s16x4 __attribute__((ext_vector_type(4)));
typedef short s16x8 __attribute__((ext_vector_type(8)));
typedef __bf16 bf2_t __attribute__((ext_vector_type(2)));
typedef float f32x4 __attribute__((ext_vector_type(4)));
typedef float f32x16 __attribute__((ext_vector_type(16)));
typedef unsigned u32x4 __attribute__((ext_vector_type(4)));
typedef unsigned u32x2 __attribute__((ext_vector_type(2)));
typedef unsigned short u16;
typedef __attribute__((address_space(3))) const char* lds_cptr;
typedef __attribute__((address_space(3))) char* lds_ptr;
typedef __attribute__((address_space(3))) s16x4* lds_s16x4_ptr;

#define DEV __device__ __forceinline__
#define MFMA32(a, b, c) __builtin_amdgcn_mfma_f32_32x32x16_bf16(a, b, c, 0, 0, 0)
#define MFMA16(a, b, c) __builtin_amdgcn_mfma_f32_16x16x32_bf16(a, b, c, 0, 0, 0)

constexpr int S_ = 2048, D_ = 576, F_ = 4608;
constexpr float EPS = 1e-5f;

DEV s16x4 vtr(lds_cptr p) { return __builtin_amdgcn_ds_read_tr16_b64_v4i16((lds_s16x4_ptr)p); }
DEV s16x8 cat8(s16x4 lo, s16x4 hi) { return __builtin_shufflevector(lo, hi, 0, 1, 2, 3, 4, 5, 6, 7); }
DEV unsigned pack_bf16(float a, float b) {
  bf2_t v; v[0] = (__bf16)a; v[1] = (__bf16)b; return __builtin_bit_cast(unsigned, v);
}
DEV float bf_lo(unsigned u) { return __builtin_bit_cast(float, u << 16); }
DEV float bf_hi(unsigned u) { return __builtin_bit_cast(float, u & 0xffff0000u); }
DEV float dot2bf(unsigned a, unsigned b, float c) {
  return __builtin_amdgcn_fdot2_f32_bf16(__builtin_bit_cast(bf2_t, a), __builtin_bit_cast(bf2_t, b), c, false);
}
DEV void glds16(unsigned voff, const void* sbase_, unsigned ldsaddr_) {
  unsigned sv;
  const uint64_t pv = (uint64_t)sbase_;
  const uint64_t sbase = ((uint64_t)(unsigned)__builtin_amdgcn_readfirstlane((int)(pv >> 32)) << 32) |
                         (uint64_t)(unsigned)__builtin_amdgcn_readfirstlane((int)(unsigned)pv);
  const unsigned ldsaddr = (unsigned)__builtin_amdgcn_readfirstlane((int)ldsaddr_);
  asm volatile("s_mov_b32 %0, m0\n\ts_mov_b32 m0, %3\n\ts_nop 0\n\tglobal_load_lds_dwordx4 %1, %2\n\ts_mov_b32 m0, %0"
               : "=&s"(sv) : "v"(voff), "s"(sbase), "s"(ldsaddr) : "memory");
}
DEV void gtouch(unsigned voff, const void* sbase_, unsigned ldsaddr_) {
  unsigned sv;
  const uint64_t pv = (uint64_t)sbase_;
  const uint64_t sbase = ((uint64_t)(unsigned)__builtin_amdgcn_readfirstlane((int)(pv >> 32)) << 32) |
                         (uint64_t)(unsigned)__builtin_amdgcn_readfirstlane((int)(unsigned)pv);
  const unsigned ldsaddr = (unsigned)__builtin_amdgcn_readfirstlane((int)ldsaddr_);
  asm volatile("s_mov_b32 %0, m0\n\ts_mov_b32 m0, %3\n\ts_nop 0\n\tglobal_load_lds_dword %1, %2\n\ts_mov_b32 m0, %0"
               : "=&s"(sv) : "v"(voff), "s"(sbase), "s"(ldsaddr) : "memory");
}
#define WAIT_BAR(N) asm volatile("s_waitcnt vmcnt(" #N ") lgkmcnt(0)\n\ts_barrier" ::: "memory")

template <int VM, int LGKM = 15> DEV void waitcnt() {
  __builtin_amdgcn_s_waitcnt((VM & 15) | ((VM >> 4) << 14) | (7 << 4) | ((LGKM & 15) << 8));
}
DEV void barrier() { asm volatile("s_barrier" ::: "memory"); }
template <int N> DEV void wait_bar() { asm volatile("s_waitcnt vmcnt(%0) lgkmcnt(0)\n\ts_barrier" ::"i"(N) : "memory"); }
template <int B, int E, class F> DEV void static_for(F&& f) {
  if constexpr (B < E) { f(std::integral_constant<int, B>{}); static_for<B + 1, E>(f); }
}
#define AINL __attribute__((always_inline))

#define FS(i) do { } while (0)
#define STAMP(k, i) do { } while (0)
DEV float gelu_tanh(float x) {
  const float c1 = -2.0f * 1.4426950408889634f * 0.7978845608028654f;
  const float c3 = c1 * 0.044715f;
  float x2 = x * x;
  float t = x * __builtin_fmaf(x2, c3, c1);
  float e = __builtin_amdgcn_exp2f(t);
  return x * __builtin_amdgcn_rcpf(1.0f + e);
}

constexpr int K1_ROW = 288;
constexpr int K1_XO = 64 * K1_ROW;
constexpr int K1_SLOT = K1_XO + 144 * 128;
constexpr int K1_SCR = 4 * K1_SLOT;
constexpr int K1_LDS = K1_SCR + 256;
constexpr int K1_QS = 232;

__global__ __launch_bounds__(512, 2) void k1_qkv_attn(const u16* __restrict__ x, const u16* __restrict__ wqkv,
                                                      u16* __restrict__ o, const u16* __restrict__ wo,
                                                      const u16* __restrict__ w1, const u16* __restrict__ w2) {
  extern __shared__ __attribute__((aligned(16))) char smem[];
  const int tid = threadIdx.x, lane = tid & 63;
  const int wid8 = __builtin_amdgcn_readfirstlane(tid >> 6);
  const int wid = wid8 & 3;
  const int b = blockIdx.x, xcd = b & 7, bi = b >> 3;
  const int hp = 2 * xcd + (bi & 1), pnl = bi >> 1;
  const int par = hp & 1;
  const int i16 = lane & 15, g = lane >> 4, q4 = i16 >> 2, p4 = i16 & 3;
  const unsigned lds0 = (unsigned)(uintptr_t)smem;
  const int T0 = pnl * 128 + wid * 32;
  STAMP(0, 0);

  if (wid8 >= 4) {
  unsigned wo_[5], xo_[5];
  {
    const int segbase = (72 * hp) & ~15;
#pragma unroll
    for (int j = 0; j < 5; ++j) {
      const int n = j < 4 ? wid + 4 * j : 16 + (wid & 1);
      const int idx = n * 64 + lane;
      const int prow = idx / 18; int ch = idx - prow * 18; ch = ch > 14 ? 14 : ch;
      const int q = (prow >> 2) & 3;
      const int row = (prow & ~15) | ((q == 1 ? 2 : (q == 2 ? 1 : q)) << 2) | (prow & 3);
      const int seg = ch / 5, cc = ch - seg * 5;
      wo_[j] = (unsigned)(row * 3456 + seg * 1152 + segbase + cc * 16);
      const int xrow = idx >> 3, xch = idx & 7;
      int tok = pnl * 128 - 16 + xrow; tok = tok < 0 ? 0 : tok;
      xo_[j] = (unsigned)(tok * 1152 + ((xch ^ (xrow & 7)) * 16));
    }
  }
  auto issue_stage = [&](int st) AINL {
    const unsigned dst = lds0 + (unsigned)((st & 3) * K1_SLOT);
    const char* wb = (const char*)wqkv + (size_t)st * 64 * 3456;
    const char* xbp = (const char*)x + st * 128;
#pragma unroll
    for (int j = 0; j < 4; ++j) {
      glds16(wo_[j], wb, dst + (unsigned)((wid + 4 * j) * 1024));
      glds16(xo_[j], xbp, dst + (unsigned)(K1_XO + (wid + 4 * j) * 1024));
    }
    if (wid < 2) glds16(wo_[4], wb, dst + (unsigned)((16 + wid) * 1024));
    else glds16(xo_[4], xbp, dst + (unsigned)(K1_XO + (16 + wid - 2) * 1024));
  };
    issue_stage(0); issue_stage(1); issue_stage(2); issue_stage(3);
    waitcnt<27>(); barrier();
#pragma unroll 1
    for (int st = 0; st < 9; ++st) {
      if (st < 6) waitcnt<18>(); else if (st == 6) waitcnt<9>(); else waitcnt<0>();
      barrier();
      if (st + 4 < 9) issue_stage(st + 4);
    }
    barrier();
    if (bi < 21) {
      int L = bi * 256 + (tid & 255); L = L > 5183 ? 5183 : L;
      const int row = L / 9, l = L - row * 9;
      const unsigned scr = lds0 + (unsigned)K1_SCR;
      gtouch((unsigned)(row * 9216 + 1152 * xcd + l * 128), w1, scr);
      gtouch((unsigned)(663552 * xcd + L * 128), w2, scr);
      gtouch((unsigned)(L * 128), wo, scr);
    }
    return;
  }
  STAMP(0, 1);

  f32x4 acc[7][3];
#pragma unroll
  for (int ft = 0; ft < 7; ++ft)
#pragma unroll
    for (int ct = 0; ct < 3; ++ct) acc[ft][ct] = f32x4{0.f, 0.f, 0.f, 0.f};
  int aoff[7];
#pragma unroll
  for (int ft = 0; ft < 7; ++ft) {
    int G = 4 * ft + p4; G = G > 26 ? 26 : G;
    const int seg = G / 9, wi = G - seg * 9;
    aoff[ft] = (16 * (g >> 1) + 4 * (g & 1) + q4) * K1_ROW + seg * 80 + par * 8 + wi * 8;
  }
  int boff[3];
#pragma unroll
  for (int ct = 0; ct < 3; ++ct) {
    const int row = wid * 32 + ct * 16 + i16;
    boff[ct] = K1_XO + row * 128 + ((g ^ (row & 7)) * 16);
  }
  f32x4 asum[3], asq[3];
#pragma unroll
  for (int ct = 0; ct < 3; ++ct) { asum[ct] = f32x4{0.f, 0.f, 0.f, 0.f}; asq[ct] = f32x4{0.f, 0.f, 0.f, 0.f}; }
  const s16x8 ones8 = {0x3f80, 0x3f80, 0x3f80, 0x3f80, 0x3f80, 0x3f80, 0x3f80, 0x3f80};
  s16x8 xbA[3], xbB[3];
  s16x4 afA[14], afB[14];
  auto load_job = [&](auto J_, auto KS_, int st, s16x8 (&xb)[3], s16x4 (&af)[14]) AINL {
    constexpr int j = decltype(J_)::value, ks = decltype(KS_)::value;
    lds_cptr sl = (lds_cptr)smem + (st & 3) * K1_SLOT;
    if constexpr (j < 3) xb[j] = *(__attribute__((address_space(3))) const s16x8*)(sl + (boff[j] ^ (ks * 64)));
    else af[j - 3] = vtr(sl + aoff[(j - 3) >> 1] + ks * 32 * K1_ROW + ((j - 3) & 1) * 8 * K1_ROW);
  };
  auto half = [&](auto KS_, int st, s16x8 (&xc)[3], s16x4 (&ac)[14], s16x8 (&xn)[3], s16x4 (&an)[14]) AINL {
    constexpr int ks = decltype(KS_)::value;
    if constexpr (ks == 1) {
      FS(3 * st + 1);
      waitcnt<63, 0>();
      barrier();
      FS(3 * st + 2);
    } else { FS(3 * st); }
    s16x8 xh;
    static_for<0, 27>([&](auto M_) AINL {
      constexpr int r = M_;
      if constexpr (r < 3) asq[r] = MFMA16(xc[r], xc[r], asq[r]);
      else if constexpr (r < 6) asum[r - 3] = MFMA16(ones8, xc[r - 3], asum[r - 3]);
      else {
        constexpr int ft = (r - 6) / 3, ct = (r - 6) % 3;
        acc[ft][ct] = MFMA16(cat8(ac[2 * ft], ac[2 * ft + 1]), ct == 0 ? xh : xc[ct], acc[ft][ct]);
      }
      if constexpr (r == 4) { xh = xc[0]; if (i16 == 0) xh = ones8; }
      {
        constexpr int u = r;
        if constexpr (u < 17) load_job(std::integral_constant<int, u>{}, std::integral_constant<int, 1 - ks>{}, st + ks, xn, an);
      }
      __builtin_amdgcn_sched_barrier(0);
    });
  };
  using KS0 = std::integral_constant<int, 0>; using KS1 = std::integral_constant<int, 1>;
  barrier();
  STAMP(0, 6);
  static_for<0, 17>([&](auto J_) AINL { load_job(J_, KS0{}, 0, xbA, afA); });
#pragma unroll 1
  for (int st = 0; st < 9; ++st) {
    half(KS0{}, st, xbA, afA, xbB, afB);
    half(KS1{}, st, xbB, afB, xbA, afA);
  }
  float ssum[3], ssq[3];
#pragma unroll
  for (int ct = 0; ct < 3; ++ct) {
    ssum[ct] = asum[ct][0];
    float dsel = (i16 & 2) ? ((i16 & 1) ? asq[ct][3] : asq[ct][2]) : ((i16 & 1) ? asq[ct][1] : asq[ct][0]);
    ssq[ct] = __shfl(dsel, (i16 >> 2) * 16 + i16);
  }
  STAMP(0, 2);
  WAIT_BAR(0);
  lds_ptr qs = (lds_ptr)smem + wid * (48 * K1_QS);
#pragma unroll
  for (int ct = 0; ct < 3; ++ct) {
    const float mean = ssum[ct] * (1.0f / 576.0f);
    float var = ssq[ct] * (1.0f / 576.0f) - mean * mean;
    var = var < 0.f ? 0.f : var;
    const float rs = 1.0f / sqrtf(var + EPS);
    const float sm = rs * mean;
#pragma unroll
    for (int ft = 0; ft < 7; ++ft) {
      const int G = 4 * ft + g;
      float v[4];
#pragma unroll
      for (int r = 0; r < 4; ++r) {
        const float c = __shfl(acc[ft][0][r], lane & 48);
        v[r] = rs * acc[ft][ct][r] - sm * c;
      }
      if (G < 27) {
        u32x2 pk = {pack_bf16(v[0], v[1]), pack_bf16(v[2], v[3])};
        *(__attribute__((address_space(3))) u32x2*)(qs + (ct * 16 + i16) * K1_QS + G * 8) = pk;
      }
    }
  }
  asm volatile("s_waitcnt lgkmcnt(0)" ::: "memory");
  STAMP(0, 3);
  {
    const int tk = lane & 31, hd = lane >> 5;
    lds_cptr qrow = (lds_cptr)qs + (16 + tk) * K1_QS + hd * 36;
    unsigned qv[9];
#pragma unroll
    for (int d = 0; d < 9; ++d) qv[d] = *(__attribute__((address_space(3))) const unsigned*)(qrow + d * 4);
    float lg[8];
#pragma unroll
    for (int w = 0; w < 8; ++w) {
      lds_cptr krow = qrow - 2 * w * K1_QS + 72;
      float a = 0.f;
#pragma unroll
      for (int d = 0; d < 9; ++d) a = dot2bf(qv[d], *(__attribute__((address_space(3))) const unsigned*)(krow + d * 4), a);
      lg[w] = (T0 + tk - 2 * w >= 0) ? a * 0.23570226039551584f : -1e9f;
    }
    float mx = lg[0];
#pragma unroll
    for (int w = 1; w < 8; ++w) mx = fmaxf(mx, lg[w]);
    float se = 0.f;
#pragma unroll
    for (int w = 0; w < 8; ++w) { lg[w] = __builtin_amdgcn_exp2f((lg[w] - mx) * 1.4426950408889634f); se += lg[w]; }
    const float inv = 1.0f / se;
    float ov[18];
#pragma unroll
    for (int d = 0; d < 18; ++d) ov[d] = 0.f;
#pragma unroll
    for (int w = 0; w < 8; ++w) {
      lds_cptr vrow = qrow - 2 * w * K1_QS + 144;
      const float pw = lg[w] * inv;
#pragma unroll
      for (int d = 0; d < 9; ++d) {
        const unsigned u = *(__attribute__((address_space(3))) const unsigned*)(vrow + d * 4);
        ov[2 * d] = __builtin_fmaf(pw, bf_lo(u), ov[2 * d]);
        ov[2 * d + 1] = __builtin_fmaf(pw, bf_hi(u), ov[2 * d + 1]);
      }
    }
    {
      const int tok = T0 + tk, c0 = hp * 36 + hd * 18;
      char* ob_ = (char*)o + (size_t)((tok >> 4) * 18 * 64 + (tok & 15)) * 16;
#pragma unroll
      for (int d = 0; d < 9; ++d) {
        const int col = c0 + 2 * d;
        *(unsigned*)(ob_ + (col >> 3) * 256 + (col & 7) * 2) = pack_bf16(ov[2 * d], ov[2 * d + 1]);
      }
    }
  }
  STAMP(0, 4); STAMP(0, 5);
}

constexpr int K2_LDS = 56 * 1024;

__global__ __launch_bounds__(256, 1) void k2_proj(const u16* __restrict__ o, const u16* __restrict__ x,
                                                  const u16* __restrict__ wo, const u16* __restrict__ w1,
                                                  u16* __restrict__ h, float* __restrict__ stats, float* __restrict__ c1) {
  extern __shared__ __attribute__((aligned(16))) char smem[];
  const int tid = threadIdx.x, lane = tid & 63;
  const int b = blockIdx.x;
  if (b >= 192) {
    const int cb = b - 192;
    const int cg = tid % 9, rg = tid / 9;
    float* red = (float*)smem;
    if (rg < 24) {
      u32x4 v[24];
#pragma unroll
      for (int i = 0; i < 24; ++i) v[i] = *(const u32x4*)(w1 + (size_t)(rg * 24 + i) * F_ + cb * 72 + cg * 8);
      float s[8];
#pragma unroll
      for (int e = 0; e < 8; ++e) s[e] = 0.f;
#pragma unroll
      for (int i = 0; i < 24; ++i)
#pragma unroll
        for (int e = 0; e < 4; ++e) { s[2 * e] += bf_lo(v[i][e]); s[2 * e + 1] += bf_hi(v[i][e]); }
#pragma unroll
      for (int e = 0; e < 8; ++e) red[rg * 72 + cg * 8 + e] = s[e];
    }
    __syncthreads();
    if (tid < 72) {
      float s = 0.f;
      for (int r = 0; r < 24; ++r) s += red[r * 72 + tid];
      c1[cb * 72 + tid] = s;
    }
    return;
  }
  const int wid = __builtin_amdgcn_readfirstlane(tid >> 6);
  STAMP(1, 0);
  const int xcd = b & 7, bi = b >> 3;
  const int pnl = 2 * xcd + (bi >= 12 ? 1 : 0), n = bi >= 12 ? bi - 12 : bi;
  const int i16 = lane & 15, g = lane >> 4, q4 = i16 >> 2, p4 = i16 & 3;
  const unsigned lds0 = (unsigned)(uintptr_t)smem;
  const int T0 = pnl * 128 + wid * 32;

  s16x8 ob[2][18];
#pragma unroll
  for (int ct = 0; ct < 2; ++ct) {
    const u32x4* op_ = (const u32x4*)o + (size_t)((T0 >> 4) + ct) * 18 * 64 + lane;
#pragma unroll
    for (int ks = 0; ks < 18; ++ks) ob[ct][ks] = __builtin_bit_cast(s16x8, op_[ks * 64]);
  }
#pragma unroll
  for (int j = 0; j < 14; ++j) {
    int ni = j * 4 + wid; ni = ni > 53 ? 53 : ni;
    const int idx = ni * 64 + lane;
    const int row = idx / 6, ch = idx - row * 6;
    glds16((unsigned)(row * 1152 + n * 96 + ch * 16), wo, lds0 + (unsigned)(ni * 1024));
  }
  u32x2 xr[2][3];
#pragma unroll
  for (int ct = 0; ct < 2; ++ct)
#pragma unroll
    for (int ft = 0; ft < 3; ++ft)
      xr[ct][ft] = *(const u32x2*)(x + (size_t)(T0 + ct * 16 + i16) * D_ + n * 48 + ft * 16 + 4 * g);

  f32x4 acc[3][2];
#pragma unroll
  for (int ft = 0; ft < 3; ++ft)
#pragma unroll
    for (int ct = 0; ct < 2; ++ct) acc[ft][ct] = f32x4{0.f, 0.f, 0.f, 0.f};
  lds_cptr abase = (lds_cptr)smem + (8 * g + q4) * 96 + p4 * 8;
  STAMP(1, 1);
  WAIT_BAR(0);
  STAMP(1, 2);
#pragma unroll
  for (int ks = 0; ks < 18; ++ks)
#pragma unroll
    for (int ft = 0; ft < 3; ++ft) {
      s16x8 a = cat8(vtr(abase + ks * 32 * 96 + ft * 32), vtr(abase + ks * 32 * 96 + ft * 32 + 4 * 96));
#pragma unroll
      for (int ct = 0; ct < 2; ++ct) acc[ft][ct] = MFMA16(a, ob[ct][ks], acc[ft][ct]);
    }
#pragma unroll
  for (int ct = 0; ct < 2; ++ct) {
    const int tok = T0 + ct * 16 + i16;
    float hv[12];
#pragma unroll
    for (int ft = 0; ft < 3; ++ft) {
      const unsigned p0 = pack_bf16(bf_lo(xr[ct][ft][0]) + acc[ft][ct][0], bf_hi(xr[ct][ft][0]) + acc[ft][ct][1]);
      const unsigned p1 = pack_bf16(bf_lo(xr[ct][ft][1]) + acc[ft][ct][2], bf_hi(xr[ct][ft][1]) + acc[ft][ct][3]);
      u32x2 pk = {p0, p1};
      {
        const int col = n * 48 + ft * 16 + 4 * g;
        const int ks = col >> 4, hh2 = (col >> 3) & 1, j4 = col & 4;
        *(u32x2*)(h + ((size_t)(((tok >> 5) * 36 + ks) * 64 + hh2 * 32 + (tok & 31)) * 8 + j4)) = pk;
      }
      hv[ft * 4 + 0] = bf_lo(p0); hv[ft * 4 + 1] = bf_hi(p0); hv[ft * 4 + 2] = bf_lo(p1); hv[ft * 4 + 3] = bf_hi(p1);
    }
    float s = 0.f;
#pragma unroll
    for (int e = 0; e < 12; ++e) s += hv[e];
    s += __shfl_xor(s, 16); s += __shfl_xor(s, 32);
    const float mean = s * (1.0f / 48.0f);
    float m2 = 0.f;
#pragma unroll
    for (int e = 0; e < 12; ++e) { const float d = hv[e] - mean; m2 = __builtin_fmaf(d, d, m2); }
    m2 += __shfl_xor(m2, 16); m2 += __shfl_xor(m2, 32);
    if (g == 0) { stats[(size_t)tok * 24 + n * 2] = s; stats[(size_t)tok * 24 + n * 2 + 1] = m2; }
  }
  STAMP(1, 3); STAMP(1, 4);
}

constexpr int HS = 18432;
constexpr int K3_XR = 4 * HS;
constexpr int K3_TAB = K3_XR + 73728;
constexpr int K3_SCR = K3_TAB + 288 * 8;
constexpr int K3_LDS = K3_SCR + 256;

__global__ __launch_bounds__(512, 2) void k3_mlp(const u32x4* __restrict__ hT, const float* __restrict__ stats,
                                                 const float* __restrict__ c1, const u16* __restrict__ w1,
                                                 const u16* __restrict__ b1, const u16* __restrict__ w2,
                                                 u32x4* __restrict__ part) {
  extern __shared__ __attribute__((aligned(16))) char smem[];
  const int tid = threadIdx.x, lane = tid & 63;
  const int wid = __builtin_amdgcn_readfirstlane(tid >> 6);
  const int b = blockIdx.x, xcd = b & 7, bi = b >> 3;
  const int c = 2 * xcd + (bi & 1), pnl = bi >> 1;
  const unsigned lds0 = (unsigned)(uintptr_t)smem;
  STAMP(2, 0);

  if (wid >= 4) {
    const int lw = wid - 4;
    unsigned o1[5], o2[5];
#pragma unroll
    for (int j = 0; j < 5; ++j) {
      const int n = j < 4 ? lw + 4 * j : 16 + (lw & 1);
      const int row = n * 16 + (lane >> 2), ch = lane & 3;
      o1[j] = (unsigned)(row * (F_ * 2) + ch * 16);
      o2[j] = (unsigned)(row * (D_ * 2) + ch * 16);
    }
    const char* w1c = (const char*)w1 + (size_t)(288 * c) * 2;
    const char* w2c = (const char*)w2 + (size_t)(288 * c) * D_ * 2;
    auto issue_step = [&](int s) AINL {
      const bool is2 = s >= 18;
      const char* base = is2 ? w2c + (size_t)(64 * (s - 18)) : w1c + ((size_t)(288 * (s & 1)) * F_ + 32 * (s >> 1)) * 2;
      const unsigned dst = lds0 + (unsigned)((s & 3) * HS);
#pragma unroll
      for (int j = 0; j < 4; ++j) glds16(is2 ? o2[j] : o1[j], base, dst + (unsigned)((lw + 4 * j) * 1024));
      if ((lw >> 1) == (s & 1)) glds16(is2 ? o2[4] : o1[4], base, dst + (unsigned)((16 + (lw & 1)) * 1024));
    };
    issue_step(0); issue_step(1); issue_step(2);
    {
      float* tc = (float*)(smem + K3_TAB);
      const int t = tid - 256;
      tc[t] = c1[288 * c + t]; tc[288 + t] = bf_lo(b1[288 * c + t]);
      if (t < 32) { tc[256 + t] = c1[288 * c + 256 + t]; tc[288 + 256 + t] = bf_lo(b1[288 * c + 256 + t]); }
    }
    waitcnt<4, 0>(); barrier();
#pragma unroll 1
    for (int s = 0; s < 36; ++s) {
      if (s + 3 < 36) issue_step(s + 3);
      if (s >= 20) {
        const int d = s - 20;
        lds_cptr sp = (lds_cptr)smem + K3_XR + lw * 18432 + (d & 1) * 2048 + lane * 16;
        const u32x4 q0 = *(__attribute__((address_space(3))) const u32x4*)sp;
        const u32x4 q1 = *(__attribute__((address_space(3))) const u32x4*)(sp + 1024);
        u32x4* dq = part + (size_t)((((c * 16 + pnl) * 4 + lw) * 36) + 2 * d) * 64 + lane;
        dq[0] = q0; dq[64] = q1;
        if (s + 3 < 36) waitcnt<6>(); else waitcnt<2>();
      } else {
        if (s + 3 < 36) waitcnt<4>(); else waitcnt<0>();
      }
      barrier();
    }
    {
      lds_cptr sp = (lds_cptr)smem + K3_XR + lw * 18432 + lane * 16;
      const u32x4 q0 = *(__attribute__((address_space(3))) const u32x4*)sp;
      const u32x4 q1 = *(__attribute__((address_space(3))) const u32x4*)(sp + 1024);
      u32x4* dq = part + (size_t)((((c * 16 + pnl) * 4 + lw) * 36) + 2 * 16) * 64 + lane;
      dq[0] = q0; dq[64] = q1;
    }
    return;
  }

  const int t32 = lane & 31, hh = lane >> 5;
  const int tok = pnl * 128 + wid * 32 + t32;
  const int i16 = lane & 15, q4 = i16 >> 2, p4 = i16 & 3, blk = (lane >> 4) & 1;
  s16x8 hb[36];
  {
    const u32x4* hp = hT + (size_t)((pnl * 4 + wid) * 36) * 64 + lane;
#pragma unroll
    for (int ks = 0; ks < 36; ++ks) hb[ks] = __builtin_bit_cast(s16x8, hp[ks * 64]);
  }
  float sc, scm;
  {
    f32x4 stv[6];
#pragma unroll
    for (int i = 0; i < 6; ++i) stv[i] = *(const f32x4*)(stats + (size_t)tok * 24 + i * 4);
    float sum = 0.f;
#pragma unroll
    for (int i = 0; i < 6; ++i) sum += stv[i][0] + stv[i][2];
    const float mean = sum * (1.0f / 576.0f);
    float m2 = 0.f;
#pragma unroll
    for (int i = 0; i < 6; ++i) {
      const float d0 = stv[i][0] * (1.0f / 48.0f) - mean, d1 = stv[i][2] * (1.0f / 48.0f) - mean;
      m2 += stv[i][1] + stv[i][3] + 48.0f * (d0 * d0 + d1 * d1);
    }
    const float var = m2 * (1.0f / 576.0f);
    const float r1 = __builtin_amdgcn_rsqf(var + EPS);
    const float var2 = var * r1 * r1;
    const float r2 = __builtin_amdgcn_rsqf(var2 + EPS);
    sc = r1 * r2; scm = sc * mean;
  }
  const int lch = (16 * blk + 4 * p4) * 2;
  lds_cptr lb1 = (lds_cptr)smem + (8 * hh + q4) * 64 + lch;
  lds_cptr lb2 = (lds_cptr)smem + (4 * hh + q4) * 64 + lch;
  lds_ptr xw = (lds_ptr)smem + K3_XR + wid * 18432 + lane * 16;

  constexpr int LA = 4;
  s16x8 fr[LA];
  f32x16 accA, accB, zero16;
#pragma unroll
  for (int r = 0; r < 16; ++r) { accA[r] = 0.f; accB[r] = 0.f; zero16[r] = 0.f; }
  using TT = std::true_type; using FF = std::false_type;
  using P0 = std::integral_constant<int, 0>; using P2 = std::integral_constant<int, 2>;

  STAMP(2, 1);
  waitcnt<0, 0>(); barrier();
  STAMP(2, 2);
  static_for<0, LA>([&](auto M) AINL {
    constexpr int m = M;
    lds_cptr a = lb1 + m * 16 * 64;
    fr[m] = cat8(vtr(a), vtr(a + 4 * 64));
  });

  auto pass1 = [&](auto LAST_, f32x16& acc, const f32x16& accp, int it) AINL {
    constexpr bool LAST = decltype(LAST_)::value;
    const int ftp = it == 0 ? 0 : it - 1;
    const int oA = ((2 * it) & 3) * HS, oN = ((2 * it + 2) & 3) * HS;
    unsigned gp[8];
    float v0 = 0.f, v1 = 0.f, u0 = 0.f, u1 = 0.f;
    f32x4 tcc, tbb;
    static_for<0, 36>([&](auto MM) AINL {
      constexpr int m = MM;
      if constexpr (m == 18) barrier();
      acc = MFMA32(fr[m % LA], hb[m], m == 0 ? zero16 : acc);
      constexpr int mn = m + LA;
      lds_cptr a = mn < 36 ? lb1 + oA + (mn / 18) * HS + (mn % 18) * 16 * 64 : (LAST ? lb2 : lb1) + oN + (mn - 36) * 16 * 64;
      constexpr int r2 = (mn >= 36 && LAST) ? 8 * 64 : 4 * 64;
      s16x4 flo, fhi;
      constexpr int ph = (m - 4) & 3, e = ((m - 4) >> 2) * 2;
      const float gc1 = -2.0f * 1.4426950408889634f * 0.7978845608028654f, gc3 = gc1 * 0.044715f;
      if constexpr (m >= 4 && ph == 0) {
        u0 = __builtin_fmaf(-scm, tcc[e & 3], tbb[e & 3]); u1 = __builtin_fmaf(-scm, tcc[(e & 3) + 1], tbb[(e & 3) + 1]);
        flo = vtr(a);
        v0 = __builtin_fmaf(sc, accp[e], u0); v1 = __builtin_fmaf(sc, accp[e + 1], u1);
        fhi = vtr(a + r2);
        u0 = v0 * v0;
      } else if constexpr (m >= 4 && ph == 1) {
        u1 = v1 * v1; u0 = __builtin_fmaf(u0, gc3, gc1);
        flo = vtr(a);
        u1 = __builtin_fmaf(u1, gc3, gc1); u0 = v0 * u0;
        fhi = vtr(a + r2);
        u1 = v1 * u1;
      } else if constexpr (m >= 4 && ph == 2) {
        u0 = __builtin_amdgcn_exp2f(u0); u1 = __builtin_amdgcn_exp2f(u1);
        flo = vtr(a); fhi = vtr(a + r2);
        u0 = 1.0f + u0; u1 = 1.0f + u1;
      } else if constexpr (m >= 4 && ph == 3) {
        u0 = __builtin_amdgcn_rcpf(u0); u1 = __builtin_amdgcn_rcpf(u1);
        flo = vtr(a); fhi = vtr(a + r2);
        gp[e >> 1] = pack_bf16(v0 * u0, v1 * u1);
        asm volatile("" : "+v"(gp[e >> 1]));
      } else { flo = vtr(a); fhi = vtr(a + r2); }
      asm volatile("" : "+v"(v0), "+v"(v1), "+v"(u0), "+v"(u1));
      if constexpr ((m & 7) == 1 && m < 32) {
        lds_cptr tc = (lds_cptr)smem + K3_TAB + (32 * ftp + 4 * hh + m - 1) * 4;
        tcc = *(__attribute__((address_space(3))) const f32x4*)tc;
        tbb = *(__attribute__((address_space(3))) const f32x4*)(tc + 288 * 4);
      }
      if constexpr (m == 20) *(__attribute__((address_space(3))) u32x4*)(xw + (ftp * 2) * 1024) = u32x4{gp[0], gp[1], gp[2], gp[3]};
      fr[m % LA] = cat8(flo, fhi);
      __builtin_amdgcn_sched_barrier(0);
    });
    *(__attribute__((address_space(3))) u32x4*)(xw + (ftp * 2 + 1) * 1024) = u32x4{gp[4], gp[5], gp[6], gp[7]};
    barrier();
  };
#pragma unroll 1
  for (int i2 = 0; i2 < 4; ++i2) {
    pass1(FF{}, accA, accB, 2 * i2);
    pass1(FF{}, accB, accA, 2 * i2 + 1);
  }
  pass1(TT{}, accA, accB, 8);
  STAMP(2, 3);
  {
    float gv[16];
#pragma unroll
    for (int rg = 0; rg < 4; ++rg) {
      lds_cptr tc = (lds_cptr)smem + K3_TAB + (32 * 8 + 4 * hh + 8 * rg) * 4;
      const f32x4 cc = *(__attribute__((address_space(3))) const f32x4*)tc;
      const f32x4 bb = *(__attribute__((address_space(3))) const f32x4*)(tc + 288 * 4);
#pragma unroll
      for (int e = 0; e < 4; ++e) gv[rg * 4 + e] = gelu_tanh(__builtin_fmaf(sc, accA[rg * 4 + e], __builtin_fmaf(-scm, cc[e], bb[e])));
    }
#pragma unroll
    for (int s = 0; s < 2; ++s) {
      u32x4 pk = {pack_bf16(gv[8 * s], gv[8 * s + 1]), pack_bf16(gv[8 * s + 2], gv[8 * s + 3]),
                  pack_bf16(gv[8 * s + 4], gv[8 * s + 5]), pack_bf16(gv[8 * s + 6], gv[8 * s + 7])};
      *(__attribute__((address_space(3))) u32x4*)(xw + (16 + s) * 1024) = pk;
    }
  }
  s16x8 xbf[18];
#pragma unroll
  for (int k = 0; k < 18; ++k) xbf[k] = *(__attribute__((address_space(3))) const s16x8*)(xw + k * 1024);
  STAMP(2, 4);

  u32x4* dst = part + (size_t)(((c * 16 + pnl) * 4 + wid) * 36) * 64 + lane;
  auto pass2 = [&](auto LAST_, auto PH_, f32x16& acc, const f32x16& accp, int d) AINL {
    constexpr bool LAST = decltype(LAST_)::value;
    constexpr int PH = decltype(PH_)::value;
    const int oA = ((18 + d) & 3) * HS, oN = ((19 + d) & 3) * HS;
    lds_ptr sw = xw + ((d + 1) & 1) * 2048;
    u32x4 pk0, pk1;
    static_for<0, 18>([&](auto MM) AINL {
      constexpr int m = MM;
      acc = MFMA32(fr[(m + PH) % LA], xbf[m], m == 0 ? zero16 : acc);
      constexpr int mn = m + LA;
      if constexpr (mn < 18) { lds_cptr a = lb2 + oA + mn * 16 * 64; fr[(m + PH) % LA] = cat8(vtr(a), vtr(a + 8 * 64)); }
      else if constexpr (!LAST) { lds_cptr a = lb2 + oN + (mn - 18) * 16 * 64; fr[(m + PH) % LA] = cat8(vtr(a), vtr(a + 8 * 64)); }
      if constexpr (m >= 2 && m < 6) { pk0[m - 2] = pack_bf16(accp[2 * (m - 2)], accp[2 * (m - 2) + 1]); pk1[m - 2] = pack_bf16(accp[8 + 2 * (m - 2)], accp[8 + 2 * (m - 2) + 1]); }
      if constexpr (m == 6) *(__attribute__((address_space(3))) u32x4*)sw = pk0;
      if constexpr (m == 7) *(__attribute__((address_space(3))) u32x4*)(sw + 1024) = pk1;
      __builtin_amdgcn_sched_barrier(0);
    });
    if constexpr (!LAST) barrier();
  };
#pragma unroll 1
  for (int d2 = 0; d2 < 8; ++d2) {
    pass2(FF{}, P0{}, accA, accB, 2 * d2);
    pass2(FF{}, P2{}, accB, accA, 2 * d2 + 1);
  }
  pass2(FF{}, P0{}, accA, accB, 16);
  pass2(TT{}, P2{}, accB, accA, 17);
  barrier();
#pragma unroll
  for (int h2 = 0; h2 < 2; ++h2) {
    u32x4 pk = {pack_bf16(accB[8 * h2], accB[8 * h2 + 1]), pack_bf16(accB[8 * h2 + 2], accB[8 * h2 + 3]),
                pack_bf16(accB[8 * h2 + 4], accB[8 * h2 + 5]), pack_bf16(accB[8 * h2 + 6], accB[8 * h2 + 7])};
    dst[(17 * 2 + h2) * 64] = pk;
  }
  STAMP(2, 5);
}

__global__ __launch_bounds__(256) void k4_reduce(const u32x4* __restrict__ part, const u16* __restrict__ h,
                                                 const u16* __restrict__ b2, u16* __restrict__ out) {
  const int gid = blockIdx.x * 256 + threadIdx.x;
  const int lane = gid & 63, rest = gid >> 6;
  const int h2 = rest & 1, dtl = (rest >> 1) & 1, r1 = rest >> 2;
  const int dp = r1 % 9, r3 = r1 / 9;
  const int t32 = lane & 31, hh = lane >> 5;
  const int tok = r3 * 32 + t32;
  const int d0 = 64 * dp + 32 * dtl + 16 * h2 + 4 * hh;
  u32x4 v[16];
#pragma unroll
  for (int c = 0; c < 16; ++c) v[c] = part[(size_t)c * 147456 + gid];
  const u16* hp = h + ((size_t)((r3 * 36 + (d0 >> 4)) * 64 + t32) * 8 + 4 * hh);
  const u32x2 ha = *(const u32x2*)hp, hb = *(const u32x2*)(hp + 256);
  const u32x2 ba = *(const u32x2*)(b2 + d0), bb = *(const u32x2*)(b2 + d0 + 8);
  float s[8];
  s[0] = bf_lo(ha[0]) + bf_lo(ba[0]); s[1] = bf_hi(ha[0]) + bf_hi(ba[0]);
  s[2] = bf_lo(ha[1]) + bf_lo(ba[1]); s[3] = bf_hi(ha[1]) + bf_hi(ba[1]);
  s[4] = bf_lo(hb[0]) + bf_lo(bb[0]); s[5] = bf_hi(hb[0]) + bf_hi(bb[0]);
  s[6] = bf_lo(hb[1]) + bf_lo(bb[1]); s[7] = bf_hi(hb[1]) + bf_hi(bb[1]);
  float a[8];
#pragma unroll
  for (int e = 0; e < 8; ++e) a[e] = 0.f;
#pragma unroll
  for (int c = 0; c < 16; ++c)
#pragma unroll
    for (int e = 0; e < 4; ++e) { a[2 * e] += bf_lo(v[c][e]); a[2 * e + 1] += bf_hi(v[c][e]); }
  u32x2 oa = {pack_bf16(s[0] + a[0], s[1] + a[1]), pack_bf16(s[2] + a[2], s[3] + a[3])};
  u32x2 ob = {pack_bf16(s[4] + a[4], s[5] + a[5]), pack_bf16(s[6] + a[6], s[7] + a[7])};
  *(u32x2*)(out + (size_t)tok * D_ + d0) = oa;
  *(u32x2*)(out + (size_t)tok * D_ + d0 + 8) = ob;
}

extern "C" void kernel_launch(void* const* d_in, const int* in_sizes, int n_in, void* d_out, int out_size,
                              void* d_ws, size_t ws_size, hipStream_t stream) {
  const u16* x = (const u16*)d_in[0];
  const u16* wqkv = (const u16*)d_in[1];
  const u16* wo = (const u16*)d_in[2];
  const u16* w1 = (const u16*)d_in[4];
  const u16* b1 = (const u16*)d_in[5];
  const u16* w2 = (const u16*)d_in[6];
  const u16* b2 = (const u16*)d_in[7];
  u16* out = (u16*)d_out;
  char* ws = (char*)d_ws;
  u16* o = (u16*)ws;
  u16* h = (u16*)(ws + 2359296);
  float* stats = (float*)(ws + 2 * 2359296);
  float* c1 = (float*)(ws + 2 * 2359296 + 196608);
  u32x4* part = (u32x4*)(ws + 2 * 2359296 + 196608 + 18432 + 2048);

  hipFuncSetAttribute((const void*)k1_qkv_attn, hipFuncAttributeMaxDynamicSharedMemorySize, K1_LDS);
  hipFuncSetAttribute((const void*)k2_proj, hipFuncAttributeMaxDynamicSharedMemorySize, K2_LDS);
  hipFuncSetAttribute((const void*)k3_mlp, hipFuncAttributeMaxDynamicSharedMemorySize, K3_LDS);

  k1_qkv_attn<<<256, 512, K1_LDS, stream>>>(x, wqkv, o, wo, w1, w2);
  k2_proj<<<256, 256, K2_LDS, stream>>>(o, x, wo, w1, h, stats, c1);
  k3_mlp<<<256, 512, K3_LDS, stream>>>((const u32x4*)h, stats, c1, w1, b1, w2, part);
  k4_reduce<<<576, 256, 0, stream>>>(part, h, b2, out);
}
```

```cpp
#include <hip/hip_runtime.h>
#include <hip/hip_bf16.h>
#include <stdint.h>
#include <type_traits>

typedef short s16x4 __attribute__((ext_vector_type(4)));
typedef short s16x8 __attribute__((ext_vector_type(8)));
typedef __bf16 bf2_t __attribute__((ext_vector_type(2)));
typedef float f32x4 __attribute__((ext_vector_type(4)));
typedef float f32x16 __attribute__((ext_vector_type(16)));
typedef unsigned u32x4 __attribute__((ext_vector_type(4)));
typedef unsigned u32x2 __attribute__((ext_vector_type(2)));
typedef unsigned short u16;
typedef __attribute__((address_space(3))) const char* lds_cptr;
typedef __attribute__((address_space(3))) char* lds_ptr;
typedef __attribute__((address_space(3))) s16x4* lds_s16x4_ptr;

#define DEV __device__ __forceinline__
#define MFMA32(a, b, c) __builtin_amdgcn_mfma_f32_32x32x16_bf16(a, b, c, 0, 0, 0)
#define MFMA16(a, b, c) __builtin_amdgcn_mfma_f32_16x16x32_bf16(a, b, c, 0, 0, 0)

constexpr int S_ = 2048, D_ = 576, F_ = 4608;
constexpr float EPS = 1e-5f;

DEV s16x4 vtr(lds_cptr p) { return __builtin_amdgcn_ds_read_tr16_b64_v4i16((lds_s16x4_ptr)p); }
DEV s16x8 cat8(s16x4 lo, s16x4 hi) { return __builtin_shufflevector(lo, hi, 0, 1, 2, 3, 4, 5, 6, 7); }
DEV unsigned pack_bf16(float a, float b) {
  bf2_t v; v[0] = (__bf16)a; v[1] = (__bf16)b; return __builtin_bit_cast(unsigned, v);
}
DEV float bf_lo(unsigned u) { return __builtin_bit_cast(float, u << 16); }
DEV float bf_hi(unsigned u) { return __builtin_bit_cast(float, u & 0xffff0000u); }
DEV float dot2bf(unsigned a, unsigned b, float c) {
  return __builtin_amdgcn_fdot2_f32_bf16(__builtin_bit_cast(bf2_t, a), __builtin_bit_cast(bf2_t, b), c, false);
}
DEV void glds16(unsigned voff, const void* sbase_, unsigned ldsaddr_) {
  unsigned sv;
  const uint64_t pv = (uint64_t)sbase_;
  const uint64_t sbase = ((uint64_t)(unsigned)__builtin_amdgcn_readfirstlane((int)(pv >> 32)) << 32) |
                         (uint64_t)(unsigned)__builtin_amdgcn_readfirstlane((int)(unsigned)pv);
  const unsigned ldsaddr = (unsigned)__builtin_amdgcn_readfirstlane((int)ldsaddr_);
  asm volatile("s_mov_b32 %0, m0\n\ts_mov_b32 m0, %3\n\ts_nop 0\n\tglobal_load_lds_dwordx4 %1, %2\n\ts_mov_b32 m0, %0"
               : "=&s"(sv) : "v"(voff), "s"(sbase), "s"(ldsaddr) : "memory");
}
DEV void gtouch(unsigned voff, const void* sbase_, unsigned ldsaddr_) {
  unsigned sv;
  const uint64_t pv = (uint64_t)sbase_;
  const uint64_t sbase = ((uint64_t)(unsigned)__builtin_amdgcn_readfirstlane((int)(pv >> 32)) << 32) |
                         (uint64_t)(unsigned)__builtin_amdgcn_readfirstlane((int)(unsigned)pv);
  const unsigned ldsaddr = (unsigned)__builtin_amdgcn_readfirstlane((int)ldsaddr_);
  asm volatile("s_mov_b32 %0, m0\n\ts_mov_b32 m0, %3\n\ts_nop 0\n\tglobal_load_lds_dword %1, %2\n\ts_mov_b32 m0, %0"
               : "=&s"(sv) : "v"(voff), "s"(sbase), "s"(ldsaddr) : "memory");
}
#define WAIT_BAR(N) asm volatile("s_waitcnt vmcnt(" #N ") lgkmcnt(0)\n\ts_barrier" ::: "memory")

template <int VM, int LGKM = 15> DEV void waitcnt() {
  __builtin_amdgcn_s_waitcnt((VM & 15) | ((VM >> 4) << 14) | (7 << 4) | ((LGKM & 15) << 8));
}
DEV void barrier() { asm volatile("s_barrier" ::: "memory"); }
template <int N> DEV void wait_bar() { asm volatile("s_waitcnt vmcnt(%0) lgkmcnt(0)\n\ts_barrier" ::"i"(N) : "memory"); }
template <int B, int E, class F> DEV void static_for(F&& f) {
  if constexpr (B < E) { f(std::integral_constant<int, B>{}); static_for<B + 1, E>(f); }
}
#define AINL __attribute__((always_inline))
#define ST_AUX 16
DEV void st_part(u32x4* base, unsigned off, u32x4 v) {
  __amdgpu_buffer_rsrc_t r = __builtin_amdgcn_make_buffer_rsrc((void*)base, 0, 16 * 147456 * 16, 0x00020000);
  __builtin_amdgcn_raw_buffer_store_b128(v, r, (int)off, 0, ST_AUX);
}

#define FS(i) do { } while (0)
#define STAMP(k, i) do { } while (0)
DEV float gelu_tanh(float x) {
  const float c1 = -2.0f * 1.4426950408889634f * 0.7978845608028654f;
  const float c3 = c1 * 0.044715f;
  float x2 = x * x;
  float t = x * __builtin_fmaf(x2, c3, c1);
  float e = __builtin_amdgcn_exp2f(t);
  return x * __builtin_amdgcn_rcpf(1.0f + e);
}

constexpr int K1_ROW = 288;
constexpr int K1_XO = 64 * K1_ROW;
constexpr int K1_SLOT = K1_XO + 144 * 128;
constexpr int K1_SCR = 4 * K1_SLOT;
constexpr int K1_LDS = K1_SCR + 256;
constexpr int K1_QS = 232;

__global__ __launch_bounds__(512, 2) void k1_qkv_attn(const u16* __restrict__ x, const u16* __restrict__ wqkv,
                                                      u16* __restrict__ o, const u16* __restrict__ wo,
                                                      const u16* __restrict__ w1, const u16* __restrict__ w2) {
  extern __shared__ __attribute__((aligned(16))) char smem[];
  const int tid = threadIdx.x, lane = tid & 63;
  const int wid8 = __builtin_amdgcn_readfirstlane(tid >> 6);
  const int wid = wid8 & 3;
  const int b = blockIdx.x, xcd = b & 7, bi = b >> 3;
  const int hp = 2 * xcd + (bi & 1), pnl = bi >> 1;
  const int par = hp & 1;
  const int i16 = lane & 15, g = lane >> 4, q4 = i16 >> 2, p4 = i16 & 3;
  const unsigned lds0 = (unsigned)(uintptr_t)smem;
  const int T0 = pnl * 128 + wid * 32;
  STAMP(0, 0);

  if (wid8 >= 4) {
  unsigned wo_[5], xo_[5];
  {
    const int segbase = (72 * hp) & ~15;
#pragma unroll
    for (int j = 0; j < 5; ++j) {
      const int n = j < 4 ? wid + 4 * j : 16 + (wid & 1);
      const int idx = n * 64 + lane;
      const int prow = idx / 18; int ch = idx - prow * 18; ch = ch > 14 ? 14 : ch;
      const int q = (prow >> 2) & 3;
      const int row = (prow & ~15) | ((q == 1 ? 2 : (q == 2 ? 1 : q)) << 2) | (prow & 3);
      const int seg = ch / 5, cc = ch - seg * 5;
      wo_[j] = (unsigned)(row * 3456 + seg * 1152 + segbase + cc * 16);
      const int xrow = idx >> 3, xch = idx & 7;
      int tok = pnl * 128 - 16 + xrow; tok = tok < 0 ? 0 : tok;
      xo_[j] = (unsigned)(tok * 1152 + ((xch ^ (xrow & 7)) * 16));
    }
  }
  auto issue_stage = [&](int st) AINL {
    const unsigned dst = lds0 + (unsigned)((st & 3) * K1_SLOT);
    const char* wb = (const char*)wqkv + (size_t)st * 64 * 3456;
    const char* xbp = (const char*)x + st * 128;
#pragma unroll
    for (int j = 0; j < 4; ++j) {
      glds16(wo_[j], wb, dst + (unsigned)((wid + 4 * j) * 1024));
      glds16(xo_[j], xbp, dst + (unsigned)(K1_XO + (wid + 4 * j) * 1024));
    }
    if (wid < 2) glds16(wo_[4], wb, dst + (unsigned)((16 + wid) * 1024));
    else glds16(xo_[4], xbp, dst + (unsigned)(K1_XO + (16 + wid - 2) * 1024));
  };
    issue_stage(0); issue_stage(1); issue_stage(2); issue_stage(3);
    waitcnt<27>(); barrier();
#pragma unroll 1
    for (int st = 0; st < 9; ++st) {
      if (st < 6) waitcnt<18>(); else if (st == 6) waitcnt<9>(); else waitcnt<0>();
      barrier();
      if (st + 4 < 9) issue_stage(st + 4);
    }
    barrier();
    if (bi < 21) {
      int L = bi * 256 + (tid & 255); L = L > 5183 ? 5183 : L;
      const int row = L / 9, l = L - row * 9;
      const unsigned scr = lds0 + (unsigned)K1_SCR;
      gtouch((unsigned)(row * 9216 + 1152 * xcd + l * 128), w1, scr);
      gtouch((unsigned)(663552 * xcd + L * 128), w2, scr);
      gtouch((unsigned)(L * 128), wo, scr);
    }
    return;
  }
  STAMP(0, 1);

  f32x4 acc[7][3];
#pragma unroll
  for (int ft = 0; ft < 7; ++ft)
#pragma unroll
    for (int ct = 0; ct < 3; ++ct) acc[ft][ct] = f32x4{0.f, 0.f, 0.f, 0.f};
  int aoff[7];
#pragma unroll
  for (int ft = 0; ft < 7; ++ft) {
    int G = 4 * ft + p4; G = G > 26 ? 26 : G;
    const int seg = G / 9, wi = G - seg * 9;
    aoff[ft] = (16 * (g >> 1) + 4 * (g & 1) + q4) * K1_ROW + seg * 80 + par * 8 + wi * 8;
  }
  int boff[3];
#pragma unroll
  for (int ct = 0; ct < 3; ++ct) {
    const int row = wid * 32 + ct * 16 + i16;
    boff[ct] = K1_XO + row * 128 + ((g ^ (row & 7)) * 16);
  }
  f32x4 asum[3], asq[3];
#pragma unroll
  for (int ct = 0; ct < 3; ++ct) { asum[ct] = f32x4{0.f, 0.f, 0.f, 0.f}; asq[ct] = f32x4{0.f, 0.f, 0.f, 0.f}; }
  const s16x8 ones8 = {0x3f80, 0x3f80, 0x3f80, 0x3f80, 0x3f80, 0x3f80, 0x3f80, 0x3f80};
  s16x8 xbA[3], xbB[3];
  s16x4 afA[14], afB[14];
  auto load_job = [&](auto J_, auto KS_, int st, s16x8 (&xb)[3], s16x4 (&af)[14]) AINL {
    constexpr int j = decltype(J_)::value, ks = decltype(KS_)::value;
    lds_cptr sl = (lds_cptr)smem + (st & 3) * K1_SLOT;
    if constexpr (j < 3) xb[j] = *(__attribute__((address_space(3))) const s16x8*)(sl + (boff[j] ^ (ks * 64)));
    else af[j - 3] = vtr(sl + aoff[(j - 3) >> 1] + ks * 32 * K1_ROW + ((j - 3) & 1) * 8 * K1_ROW);
  };
  auto half = [&](auto KS_, int st, s16x8 (&xc)[3], s16x4 (&ac)[14], s16x8 (&xn)[3], s16x4 (&an)[14]) AINL {
    constexpr int ks = decltype(KS_)::value;
    if constexpr (ks == 1) {
      FS(3 * st + 1);
      waitcnt<63, 0>();
      barrier();
      FS(3 * st + 2);
    } else { FS(3 * st); }
    s16x8 xh;
    static_for<0, 27>([&](auto M_) AINL {
      constexpr int r = M_;
      if constexpr (r < 3) asq[r] = MFMA16(xc[r], xc[r], asq[r]);
      else if constexpr (r < 6) asum[r - 3] = MFMA16(ones8, xc[r - 3], asum[r - 3]);
      else {
        constexpr int ft = (r - 6) / 3, ct = (r - 6) % 3;
        acc[ft][ct] = MFMA16(cat8(ac[2 * ft], ac[2 * ft + 1]), ct == 0 ? xh : xc[ct], acc[ft][ct]);
      }
      if constexpr (r == 4) { xh = xc[0]; if (i16 == 0) xh = ones8; }
      {
        constexpr int u = r;
        if constexpr (u < 17) load_job(std::integral_constant<int, u>{}, std::integral_constant<int, 1 - ks>{}, st + ks, xn, an);
      }
      __builtin_amdgcn_sched_barrier(0);
    });
  };
  using KS0 = std::integral_constant<int, 0>; using KS1 = std::integral_constant<int, 1>;
  barrier();
  STAMP(0, 6);
  static_for<0, 17>([&](auto J_) AINL { load_job(J_, KS0{}, 0, xbA, afA); });
#pragma unroll 1
  for (int st = 0; st < 9; ++st) {
    half(KS0{}, st, xbA, afA, xbB, afB);
    half(KS1{}, st, xbB, afB, xbA, afA);
  }
  float ssum[3], ssq[3];
#pragma unroll
  for (int ct = 0; ct < 3; ++ct) {
    ssum[ct] = asum[ct][0];
    float dsel = (i16 & 2) ? ((i16 & 1) ? asq[ct][3] : asq[ct][2]) : ((i16 & 1) ? asq[ct][1] : asq[ct][0]);
    ssq[ct] = __shfl(dsel, (i16 >> 2) * 16 + i16);
  }
  STAMP(0, 2);
  WAIT_BAR(0);
  lds_ptr qs = (lds_ptr)smem + wid * (48 * K1_QS);
#pragma unroll
  for (int ct = 0; ct < 3; ++ct) {
    const float mean = ssum[ct] * (1.0f / 576.0f);
    float var = ssq[ct] * (1.0f / 576.0f) - mean * mean;
    var = var < 0.f ? 0.f : var;
    const float rs = 1.0f / sqrtf(var + EPS);
    const float sm = rs * mean;
#pragma unroll
    for (int ft = 0; ft < 7; ++ft) {
      const int G = 4 * ft + g;
      float v[4];
#pragma unroll
      for (int r = 0; r < 4; ++r) {
        const float c = __shfl(acc[ft][0][r], lane & 48);
        v[r] = rs * acc[ft][ct][r] - sm * c;
      }
      if (G < 27) {
        u32x2 pk = {pack_bf16(v[0], v[1]), pack_bf16(v[2], v[3])};
        *(__attribute__((address_space(3))) u32x2*)(qs + (ct * 16 + i16) * K1_QS + G * 8) = pk;
      }
    }
  }
  asm volatile("s_waitcnt lgkmcnt(0)" ::: "memory");
  STAMP(0, 3);
  {
    const int tk = lane & 31, hd = lane >> 5;
    lds_cptr qrow = (lds_cptr)qs + (16 + tk) * K1_QS + hd * 36;
    unsigned qv[9];
#pragma unroll
    for (int d = 0; d < 9; ++d) qv[d] = *(__attribute__((address_space(3))) const unsigned*)(qrow + d * 4);
    float lg[8];
#pragma unroll
    for (int w = 0; w < 8; ++w) {
      lds_cptr krow = qrow - 2 * w * K1_QS + 72;
      float a = 0.f;
#pragma unroll
      for (int d = 0; d < 9; ++d) a = dot2bf(qv[d], *(__attribute__((address_space(3))) const unsigned*)(krow + d * 4), a);
      lg[w] = (T0 + tk - 2 * w >= 0) ? a * 0.23570226039551584f : -1e9f;
    }
    float mx = lg[0];
#pragma unroll
    for (int w = 1; w < 8; ++w) mx = fmaxf(mx, lg[w]);
    float se = 0.f;
#pragma unroll
    for (int w = 0; w < 8; ++w) { lg[w] = __builtin_amdgcn_exp2f((lg[w] - mx) * 1.4426950408889634f); se += lg[w]; }
    const float inv = 1.0f / se;
    float ov[18];
#pragma unroll
    for (int d = 0; d < 18; ++d) ov[d] = 0.f;
#pragma unroll
    for (int w = 0; w < 8; ++w) {
      lds_cptr vrow = qrow - 2 * w * K1_QS + 144;
      const float pw = lg[w] * inv;
#pragma unroll
      for (int d = 0; d < 9; ++d) {
        const unsigned u = *(__attribute__((address_space(3))) const unsigned*)(vrow + d * 4);
        ov[2 * d] = __builtin_fmaf(pw, bf_lo(u), ov[2 * d]);
        ov[2 * d + 1] = __builtin_fmaf(pw, bf_hi(u), ov[2 * d + 1]);
      }
    }
    {
      const int tok = T0 + tk, c0 = hp * 36 + hd * 18;
      char* ob_ = (char*)o + (size_t)((tok >> 4) * 18 * 64 + (tok & 15)) * 16;
#pragma unroll
      for (int d = 0; d < 9; ++d) {
        const int col = c0 + 2 * d;
        *(unsigned*)(ob_ + (col >> 3) * 256 + (col & 7) * 2) = pack_bf16(ov[2 * d], ov[2 * d + 1]);
      }
    }
  }
  STAMP(0, 4); STAMP(0, 5);
}

constexpr int K2_LDS = 56 * 1024;

__global__ __launch_bounds__(256, 1) void k2_proj(const u16* __restrict__ o, const u16* __restrict__ x,
                                                  const u16* __restrict__ wo, const u16* __restrict__ w1,
                                                  u16* __restrict__ h, float* __restrict__ stats, float* __restrict__ c1) {
  extern __shared__ __attribute__((aligned(16))) char smem[];
  const int tid = threadIdx.x, lane = tid & 63;
  const int b = blockIdx.x;
  if (b >= 192) {
    const int cb = b - 192;
    const int cg = tid % 9, rg = tid / 9;
    float* red = (float*)smem;
    if (rg < 24) {
      u32x4 v[24];
#pragma unroll
      for (int i = 0; i < 24; ++i) v[i] = *(const u32x4*)(w1 + (size_t)(rg * 24 + i) * F_ + cb * 72 + cg * 8);
      float s[8];
#pragma unroll
      for (int e = 0; e < 8; ++e) s[e] = 0.f;
#pragma unroll
      for (int i = 0; i < 24; ++i)
#pragma unroll
        for (int e = 0; e < 4; ++e) { s[2 * e] += bf_lo(v[i][e]); s[2 * e + 1] += bf_hi(v[i][e]); }
#pragma unroll
      for (int e = 0; e < 8; ++e) red[rg * 72 + cg * 8 + e] = s[e];
    }
    __syncthreads();
    if (tid < 72) {
      float s = 0.f;
      for (int r = 0; r < 24; ++r) s += red[r * 72 + tid];
      c1[cb * 72 + tid] = s;
    }
    return;
  }
  const int wid = __builtin_amdgcn_readfirstlane(tid >> 6);
  STAMP(1, 0);
  const int xcd = b & 7, bi = b >> 3;
  const int pnl = 2 * xcd + (bi >= 12 ? 1 : 0), n = bi >= 12 ? bi - 12 : bi;
  const int i16 = lane & 15, g = lane >> 4, q4 = i16 >> 2, p4 = i16 & 3;
  const unsigned lds0 = (unsigned)(uintptr_t)smem;
  const int T0 = pnl * 128 + wid * 32;

  s16x8 ob[2][18];
#pragma unroll
  for (int ct = 0; ct < 2; ++ct) {
    const u32x4* op_ = (const u32x4*)o + (size_t)((T0 >> 4) + ct) * 18 * 64 + lane;
#pragma unroll
    for (int ks = 0; ks < 18; ++ks) ob[ct][ks] = __builtin_bit_cast(s16x8, op_[ks * 64]);
  }
#pragma unroll
  for (int j = 0; j < 14; ++j) {
    int ni = j * 4 + wid; ni = ni > 53 ? 53 : ni;
    const int idx = ni * 64 + lane;
    const int row = idx / 6, ch = idx - row * 6;
    glds16((unsigned)(row * 1152 + n * 96 + ch * 16), wo, lds0 + (unsigned)(ni * 1024));
  }
  u32x2 xr[2][3];
#pragma unroll
  for (int ct = 0; ct < 2; ++ct)
#pragma unroll
    for (int ft = 0; ft < 3; ++ft)
      xr[ct][ft] = *(const u32x2*)(x + (size_t)(T0 + ct * 16 + i16) * D_ + n * 48 + ft * 16 + 4 * g);

  f32x4 acc[3][2];
#pragma unroll
  for (int ft = 0; ft < 3; ++ft)
#pragma unroll
    for (int ct = 0; ct < 2; ++ct) acc[ft][ct] = f32x4{0.f, 0.f, 0.f, 0.f};
  lds_cptr abase = (lds_cptr)smem + (8 * g + q4) * 96 + p4 * 8;
  STAMP(1, 1);
  WAIT_BAR(0);
  STAMP(1, 2);
#pragma unroll
  for (int ks = 0; ks < 18; ++ks)
#pragma unroll
    for (int ft = 0; ft < 3; ++ft) {
      s16x8 a = cat8(vtr(abase + ks * 32 * 96 + ft * 32), vtr(abase + ks * 32 * 96 + ft * 32 + 4 * 96));
#pragma unroll
      for (int ct = 0; ct < 2; ++ct) acc[ft][ct] = MFMA16(a, ob[ct][ks], acc[ft][ct]);
    }
#pragma unroll
  for (int ct = 0; ct < 2; ++ct) {
    const int tok = T0 + ct * 16 + i16;
    float hv[12];
#pragma unroll
    for (int ft = 0; ft < 3; ++ft) {
      const unsigned p0 = pack_bf16(bf_lo(xr[ct][ft][0]) + acc[ft][ct][0], bf_hi(xr[ct][ft][0]) + acc[ft][ct][1]);
      const unsigned p1 = pack_bf16(bf_lo(xr[ct][ft][1]) + acc[ft][ct][2], bf_hi(xr[ct][ft][1]) + acc[ft][ct][3]);
      u32x2 pk = {p0, p1};
      {
        const int col = n * 48 + ft * 16 + 4 * g;
        const int ks = col >> 4, hh2 = (col >> 3) & 1, j4 = col & 4;
        *(u32x2*)(h + ((size_t)(((tok >> 5) * 36 + ks) * 64 + hh2 * 32 + (tok & 31)) * 8 + j4)) = pk;
      }
      hv[ft * 4 + 0] = bf_lo(p0); hv[ft * 4 + 1] = bf_hi(p0); hv[ft * 4 + 2] = bf_lo(p1); hv[ft * 4 + 3] = bf_hi(p1);
    }
    float s = 0.f;
#pragma unroll
    for (int e = 0; e < 12; ++e) s += hv[e];
    s += __shfl_xor(s, 16); s += __shfl_xor(s, 32);
    const float mean = s * (1.0f / 48.0f);
    float m2 = 0.f;
#pragma unroll
    for (int e = 0; e < 12; ++e) { const float d = hv[e] - mean; m2 = __builtin_fmaf(d, d, m2); }
    m2 += __shfl_xor(m2, 16); m2 += __shfl_xor(m2, 32);
    if (g == 0) { stats[(size_t)tok * 24 + n * 2] = s; stats[(size_t)tok * 24 + n * 2 + 1] = m2; }
  }
  STAMP(1, 3); STAMP(1, 4);
}

constexpr int HS = 18432;
constexpr int K3_XR = 4 * HS;
constexpr int K3_TAB = K3_XR + 73728;
constexpr int K3_SCR = K3_TAB + 288 * 8;
constexpr int K3_LDS = K3_SCR + 256;

__global__ __launch_bounds__(512, 2) void k3_mlp(const u32x4* __restrict__ hT, const float* __restrict__ stats,
                                                 const float* __restrict__ c1, const u16* __restrict__ w1,
                                                 const u16* __restrict__ b1, const u16* __restrict__ w2,
                                                 u32x4* __restrict__ part) {
  extern __shared__ __attribute__((aligned(16))) char smem[];
  const int tid = threadIdx.x, lane = tid & 63;
  const int wid = __builtin_amdgcn_readfirstlane(tid >> 6);
  const int b = blockIdx.x, xcd = b & 7, bi = b >> 3;
  const int c = 2 * xcd + (bi & 1), pnl = bi >> 1;
  const unsigned lds0 = (unsigned)(uintptr_t)smem;
  STAMP(2, 0);

  if (wid >= 4) {
    const int lw = wid - 4;
    unsigned o1[5], o2[5];
#pragma unroll
    for (int j = 0; j < 5; ++j) {
      const int n = j < 4 ? lw + 4 * j : 16 + (lw & 1);
      const int row = n * 16 + (lane >> 2), ch = lane & 3;
      o1[j] = (unsigned)(row * (F_ * 2) + ch * 16);
      o2[j] = (unsigned)(row * (D_ * 2) + ch * 16);
    }
    const char* w1c = (const char*)w1 + (size_t)(288 * c) * 2;
    const char* w2c = (const char*)w2 + (size_t)(288 * c) * D_ * 2;
    auto issue_step = [&](int s) AINL {
      const bool is2 = s >= 18;
      const char* base = is2 ? w2c + (size_t)(64 * (s - 18)) : w1c + ((size_t)(288 * (s & 1)) * F_ + 32 * (s >> 1)) * 2;
      const unsigned dst = lds0 + (unsigned)((s & 3) * HS);
#pragma unroll
      for (int j = 0; j < 4; ++j) glds16(is2 ? o2[j] : o1[j], base, dst + (unsigned)((lw + 4 * j) * 1024));
      if ((lw >> 1) == (s & 1)) glds16(is2 ? o2[4] : o1[4], base, dst + (unsigned)((16 + (lw & 1)) * 1024));
    };
    issue_step(0); issue_step(1); issue_step(2);
    {
      float* tc = (float*)(smem + K3_TAB);
      const int t = tid - 256;
      tc[t] = c1[288 * c + t]; tc[288 + t] = bf_lo(b1[288 * c + t]);
      if (t < 32) { tc[256 + t] = c1[288 * c + 256 + t]; tc[288 + 256 + t] = bf_lo(b1[288 * c + 256 + t]); }
    }
    waitcnt<4, 0>(); barrier();
#pragma unroll 1
    for (int s = 0; s < 36; ++s) {
      if (s + 3 < 36) issue_step(s + 3);
      if (s >= 20) {
        const int d = s - 20;
        lds_cptr sp = (lds_cptr)smem + K3_XR + lw * 18432 + (d & 1) * 2048 + lane * 16;
        const u32x4 q0 = *(__attribute__((address_space(3))) const u32x4*)sp;
        const u32x4 q1 = *(__attribute__((address_space(3))) const u32x4*)(sp + 1024);
        const unsigned dq = (unsigned)(((((c * 16 + pnl) * 4 + lw) * 36) + 2 * d) * 64 + lane) * 16u;
        st_part(part, dq, q0); st_part(part, dq + 1024, q1);
        if (s + 3 < 36) waitcnt<6>(); else waitcnt<2>();
      } else {
        if (s + 3 < 36) waitcnt<4>(); else waitcnt<0>();
      }
      barrier();
    }
    {
      lds_cptr sp = (lds_cptr)smem + K3_XR + lw * 18432 + lane * 16;
      const u32x4 q0 = *(__attribute__((address_space(3))) const u32x4*)sp;
      const u32x4 q1 = *(__attribute__((address_space(3))) const u32x4*)(sp + 1024);
      const unsigned dq = (unsigned)(((((c * 16 + pnl) * 4 + lw) * 36) + 2 * 16) * 64 + lane) * 16u;
      st_part(part, dq, q0); st_part(part, dq + 1024, q1);
    }
    return;
  }

  const int t32 = lane & 31, hh = lane >> 5;
  const int tok = pnl * 128 + wid * 32 + t32;
  const int i16 = lane & 15, q4 = i16 >> 2, p4 = i16 & 3, blk = (lane >> 4) & 1;
  s16x8 hb[36];
  {
    const u32x4* hp = hT + (size_t)((pnl * 4 + wid) * 36) * 64 + lane;
#pragma unroll
    for (int ks = 0; ks < 36; ++ks) hb[ks] = __builtin_bit_cast(s16x8, hp[ks * 64]);
  }
  float sc, scm;
  {
    f32x4 stv[6];
#pragma unroll
    for (int i = 0; i < 6; ++i) stv[i] = *(const f32x4*)(stats + (size_t)tok * 24 + i * 4);
    float sum = 0.f;
#pragma unroll
    for (int i = 0; i < 6; ++i) sum += stv[i][0] + stv[i][2];
    const float mean = sum * (1.0f / 576.0f);
    float m2 = 0.f;
#pragma unroll
    for (int i = 0; i < 6; ++i) {
      const float d0 = stv[i][0] * (1.0f / 48.0f) - mean, d1 = stv[i][2] * (1.0f / 48.0f) - mean;
      m2 += stv[i][1] + stv[i][3] + 48.0f * (d0 * d0 + d1 * d1);
    }
    const float var = m2 * (1.0f / 576.0f);
    const float r1 = __builtin_amdgcn_rsqf(var + EPS);
    const float var2 = var * r1 * r1;
    const float r2 = __builtin_amdgcn_rsqf(var2 + EPS);
    sc = r1 * r2; scm = sc * mean;
  }
  const int lch = (16 * blk + 4 * p4) * 2;
  lds_cptr lb1 = (lds_cptr)smem + (8 * hh + q4) * 64 + lch;
  lds_cptr lb2 = (lds_cptr)smem + (4 * hh + q4) * 64 + lch;
  lds_ptr xw = (lds_ptr)smem + K3_XR + wid * 18432 + lane * 16;

  constexpr int LA = 4;
  s16x8 fr[LA];
  f32x16 accA, accB, zero16;
#pragma unroll
  for (int r = 0; r < 16; ++r) { accA[r] = 0.f; accB[r] = 0.f; zero16[r] = 0.f; }
  using TT = std::true_type; using FF = std::false_type;
  using P0 = std::integral_constant<int, 0>; using P2 = std::integral_constant<int, 2>;

  STAMP(2, 1);
  waitcnt<0, 0>(); barrier();
  STAMP(2, 2);
  static_for<0, LA>([&](auto M) AINL {
    constexpr int m = M;
    lds_cptr a = lb1 + m * 16 * 64;
    fr[m] = cat8(vtr(a), vtr(a + 4 * 64));
  });

  auto pass1 = [&](auto LAST_, f32x16& acc, const f32x16& accp, int it) AINL {
    constexpr bool LAST = decltype(LAST_)::value;
    const int ftp = it == 0 ? 0 : it - 1;
    const int oA = ((2 * it) & 3) * HS, oN = ((2 * it + 2) & 3) * HS;
    unsigned gp[8];
    float v0 = 0.f, v1 = 0.f, u0 = 0.f, u1 = 0.f;
    f32x4 tcc, tbb;
    static_for<0, 36>([&](auto MM) AINL {
      constexpr int m = MM;
      if constexpr (m == 18) barrier();
      acc = MFMA32(fr[m % LA], hb[m], m == 0 ? zero16 : acc);
      constexpr int mn = m + LA;
      lds_cptr a = mn < 36 ? lb1 + oA + (mn / 18) * HS + (mn % 18) * 16 * 64 : (LAST ? lb2 : lb1) + oN + (mn - 36) * 16 * 64;
      constexpr int r2 = (mn >= 36 && LAST) ? 8 * 64 : 4 * 64;
      s16x4 flo, fhi;
      constexpr int ph = (m - 4) & 3, e = ((m - 4) >> 2) * 2;
      const float gc1 = -2.0f * 1.4426950408889634f * 0.7978845608028654f, gc3 = gc1 * 0.044715f;
      if constexpr (m >= 4 && ph == 0) {
        u0 = __builtin_fmaf(-scm, tcc[e & 3], tbb[e & 3]); u1 = __builtin_fmaf(-scm, tcc[(e & 3) + 1], tbb[(e & 3) + 1]);
        flo = vtr(a);
        v0 = __builtin_fmaf(sc, accp[e], u0); v1 = __builtin_fmaf(sc, accp[e + 1], u1);
        fhi = vtr(a + r2);
        u0 = v0 * v0;
      } else if constexpr (m >= 4 && ph == 1) {
        u1 = v1 * v1; u0 = __builtin_fmaf(u0, gc3, gc1);
        flo = vtr(a);
        u1 = __builtin_fmaf(u1, gc3, gc1); u0 = v0 * u0;
        fhi = vtr(a + r2);
        u1 = v1 * u1;
      } else if constexpr (m >= 4 && ph == 2) {
        u0 = __builtin_amdgcn_exp2f(u0); u1 = __builtin_amdgcn_exp2f(u1);
        flo = vtr(a); fhi = vtr(a + r2);
        u0 = 1.0f + u0; u1 = 1.0f + u1;
      } else if constexpr (m >= 4 && ph == 3) {
        u0 = __builtin_amdgcn_rcpf(u0); u1 = __builtin_amdgcn_rcpf(u1);
        flo = vtr(a); fhi = vtr(a + r2);
        gp[e >> 1] = pack_bf16(v0 * u0, v1 * u1);
        asm volatile("" : "+v"(gp[e >> 1]));
      } else { flo = vtr(a); fhi = vtr(a + r2); }
      asm volatile("" : "+v"(v0), "+v"(v1), "+v"(u0), "+v"(u1));
      if constexpr ((m & 7) == 1 && m < 32) {
        lds_cptr tc = (lds_cptr)smem + K3_TAB + (32 * ftp + 4 * hh + m - 1) * 4;
        tcc = *(__attribute__((address_space(3))) const f32x4*)tc;
        tbb = *(__attribute__((address_space(3))) const f32x4*)(tc + 288 * 4);
      }
      if constexpr (m == 20) *(__attribute__((address_space(3))) u32x4*)(xw + (ftp * 2) * 1024) = u32x4{gp[0], gp[1], gp[2], gp[3]};
      fr[m % LA] = cat8(flo, fhi);
      __builtin_amdgcn_sched_barrier(0);
    });
    *(__attribute__((address_space(3))) u32x4*)(xw + (ftp * 2 + 1) * 1024) = u32x4{gp[4], gp[5], gp[6], gp[7]};
    barrier();
  };
#pragma unroll 1
  for (int i2 = 0; i2 < 4; ++i2) {
    pass1(FF{}, accA, accB, 2 * i2);
    pass1(FF{}, accB, accA, 2 * i2 + 1);
  }
  pass1(TT{}, accA, accB, 8);
  STAMP(2, 3);
  {
    float gv[16];
#pragma unroll
    for (int rg = 0; rg < 4; ++rg) {
      lds_cptr tc = (lds_cptr)smem + K3_TAB + (32 * 8 + 4 * hh + 8 * rg) * 4;
      const f32x4 cc = *(__attribute__((address_space(3))) const f32x4*)tc;
      const f32x4 bb = *(__attribute__((address_space(3))) const f32x4*)(tc + 288 * 4);
#pragma unroll
      for (int e = 0; e < 4; ++e) gv[rg * 4 + e] = gelu_tanh(__builtin_fmaf(sc, accA[rg * 4 + e], __builtin_fmaf(-scm, cc[e], bb[e])));
    }
#pragma unroll
    for (int s = 0; s < 2; ++s) {
      u32x4 pk = {pack_bf16(gv[8 * s], gv[8 * s + 1]), pack_bf16(gv[8 * s + 2], gv[8 * s + 3]),
                  pack_bf16(gv[8 * s + 4], gv[8 * s + 5]), pack_bf16(gv[8 * s + 6], gv[8 * s + 7])};
      *(__attribute__((address_space(3))) u32x4*)(xw + (16 + s) * 1024) = pk;
    }
  }
  s16x8 xbf[18];
#pragma unroll
  for (int k = 0; k < 18; ++k) xbf[k] = *(__attribute__((address_space(3))) const s16x8*)(xw + k * 1024);
  STAMP(2, 4);

  u32x4* dst = part + (size_t)(((c * 16 + pnl) * 4 + wid) * 36) * 64 + lane;
  auto pass2 = [&](auto LAST_, auto PH_, f32x16& acc, const f32x16& accp, int d) AINL {
    constexpr bool LAST = decltype(LAST_)::value;
    constexpr int PH = decltype(PH_)::value;
    const int oA = ((18 + d) & 3) * HS, oN = ((19 + d) & 3) * HS;
    lds_ptr sw = xw + ((d + 1) & 1) * 2048;
    u32x4 pk0, pk1;
    static_for<0, 18>([&](auto MM) AINL {
      constexpr int m = MM;
      acc = MFMA32(fr[(m + PH) % LA], xbf[m], m == 0 ? zero16 : acc);
      constexpr int mn = m + LA;
      if constexpr (mn < 18) { lds_cptr a = lb2 + oA + mn * 16 * 64; fr[(m + PH) % LA] = cat8(vtr(a), vtr(a + 8 * 64)); }
      else if constexpr (!LAST) { lds_cptr a = lb2 + oN + (mn - 18) * 16 * 64; fr[(m + PH) % LA] = cat8(vtr(a), vtr(a + 8 * 64)); }
      if constexpr (m >= 2 && m < 6) { pk0[m - 2] = pack_bf16(accp[2 * (m - 2)], accp[2 * (m - 2) + 1]); pk1[m - 2] = pack_bf16(accp[8 + 2 * (m - 2)], accp[8 + 2 * (m - 2) + 1]); }
      if constexpr (m == 6) *(__attribute__((address_space(3))) u32x4*)sw = pk0;
      if constexpr (m == 7) *(__attribute__((address_space(3))) u32x4*)(sw + 1024) = pk1;
      __builtin_amdgcn_sched_barrier(0);
    });
    if constexpr (!LAST) barrier();
  };
#pragma unroll 1
  for (int d2 = 0; d2 < 8; ++d2) {
    pass2(FF{}, P0{}, accA, accB, 2 * d2);
    pass2(FF{}, P2{}, accB, accA, 2 * d2 + 1);
  }
  pass2(FF{}, P0{}, accA, accB, 16);
  pass2(TT{}, P2{}, accB, accA, 17);
  barrier();
#pragma unroll
  for (int h2 = 0; h2 < 2; ++h2) {
    u32x4 pk = {pack_bf16(accB[8 * h2], accB[8 * h2 + 1]), pack_bf16(accB[8 * h2 + 2], accB[8 * h2 + 3]),
                pack_bf16(accB[8 * h2 + 4], accB[8 * h2 + 5]), pack_bf16(accB[8 * h2 + 6], accB[8 * h2 + 7])};
    st_part(part, (unsigned)((((c * 16 + pnl) * 4 + wid) * 36 + 17 * 2 + h2) * 64 + lane) * 16u, pk);
  }
  STAMP(2, 5);
}

__global__ __launch_bounds__(256) void k4_reduce(const u32x4* __restrict__ part, const u16* __restrict__ h,
                                                 const u16* __restrict__ b2, u16* __restrict__ out) {
  const int gid = blockIdx.x * 256 + threadIdx.x;
  const int lane = gid & 63, rest = gid >> 6;
  const int h2 = rest & 1, dtl = (rest >> 1) & 1, r1 = rest >> 2;
  const int dp = r1 % 9, r3 = r1 / 9;
  const int t32 = lane & 31, hh = lane >> 5;
  const int tok = r3 * 32 + t32;
  const int d0 = 64 * dp + 32 * dtl + 16 * h2 + 4 * hh;
  u32x4 v[16];
#pragma unroll
  for (int c = 0; c < 16; ++c) v[c] = part[(size_t)c * 147456 + gid];
  const u16* hp = h + ((size_t)((r3 * 36 + (d0 >> 4)) * 64 + t32) * 8 + 4 * hh);
  const u32x2 ha = *(const u32x2*)hp, hb = *(const u32x2*)(hp + 256);
  const u32x2 ba = *(const u32x2*)(b2 + d0), bb = *(const u32x2*)(b2 + d0 + 8);
  float s[8];
  s[0] = bf_lo(ha[0]) + bf_lo(ba[0]); s[1] = bf_hi(ha[0]) + bf_hi(ba[0]);
  s[2] = bf_lo(ha[1]) + bf_lo(ba[1]); s[3] = bf_hi(ha[1]) + bf_hi(ba[1]);
  s[4] = bf_lo(hb[0]) + bf_lo(bb[0]); s[5] = bf_hi(hb[0]) + bf_hi(bb[0]);
  s[6] = bf_lo(hb[1]) + bf_lo(bb[1]); s[7] = bf_hi(hb[1]) + bf_hi(bb[1]);
  float a[8];
#pragma unroll
  for (int e = 0; e < 8; ++e) a[e] = 0.f;
#pragma unroll
  for (int c = 0; c < 16; ++c)
#pragma unroll
    for (int e = 0; e < 4; ++e) { a[2 * e] += bf_lo(v[c][e]); a[2 * e + 1] += bf_hi(v[c][e]); }
  u32x2 oa = {pack_bf16(s[0] + a[0], s[1] + a[1]), pack_bf16(s[2] + a[2], s[3] + a[3])};
  u32x2 ob = {pack_bf16(s[4] + a[4], s[5] + a[5]), pack_bf16(s[6] + a[6], s[7] + a[7])};
  *(u32x2*)(out + (size_t)tok * D_ + d0) = oa;
  *(u32x2*)(out + (size_t)tok * D_ + d0 + 8) = ob;
}

extern "C" void kernel_launch(void* const* d_in, const int* in_sizes, int n_in, void* d_out, int out_size,
                              void* d_ws, size_t ws_size, hipStream_t stream) {
  const u16* x = (const u16*)d_in[0];
  const u16* wqkv = (const u16*)d_in[1];
  const u16* wo = (const u16*)d_in[2];
  const u16* w1 = (const u16*)d_in[4];
  const u16* b1 = (const u16*)d_in[5];
  const u16* w2 = (const u16*)d_in[6];
  const u16* b2 = (const u16*)d_in[7];
  u16* out = (u16*)d_out;
  char* ws = (char*)d_ws;
  u16* o = (u16*)ws;
  u16* h = (u16*)(ws + 2359296);
  float* stats = (float*)(ws + 2 * 2359296);
  float* c1 = (float*)(ws + 2 * 2359296 + 196608);
  u32x4* part = (u32x4*)(ws + 2 * 2359296 + 196608 + 18432 + 2048);

  hipFuncSetAttribute((const void*)k1_qkv_attn, hipFuncAttributeMaxDynamicSharedMemorySize, K1_LDS);
  hipFuncSetAttribute((const void*)k2_proj, hipFuncAttributeMaxDynamicSharedMemorySize, K2_LDS);
  hipFuncSetAttribute((const void*)k3_mlp, hipFuncAttributeMaxDynamicSharedMemorySize, K3_LDS);

  k1_qkv_attn<<<256, 512, K1_LDS, stream>>>(x, wqkv, o, wo, w1, w2);
  k2_proj<<<256, 256, K2_LDS, stream>>>(o, x, wo, w1, h, stats, c1);
  k3_mlp<<<256, 512, K3_LDS, stream>>>((const u32x4*)h, stats, c1, w1, b1, w2, part);
  k4_reduce<<<576, 256, 0, stream>>>(part, h, b2, out);
}
```

```cpp
#include <hip/hip_runtime.h>
#include <hip/hip_bf16.h>
#include <stdint.h>
#include <type_traits>

typedef short s16x4 __attribute__((ext_vector_type(4)));
typedef short s16x8 __attribute__((ext_vector_type(8)));
typedef __bf16 bf2_t __attribute__((ext_vector_type(2)));
typedef float f32x4 __attribute__((ext_vector_type(4)));
typedef float f32x16 __attribute__((ext_vector_type(16)));
typedef unsigned u32x4 __attribute__((ext_vector_type(4)));
typedef unsigned u32x2 __attribute__((ext_vector_type(2)));
typedef unsigned short u16;
typedef __attribute__((address_space(3))) const char* lds_cptr;
typedef __attribute__((address_space(3))) char* lds_ptr;
typedef __attribute__((address_space(3))) s16x4* lds_s16x4_ptr;

#define DEV __device__ __forceinline__
#define MFMA32(a, b, c) __builtin_amdgcn_mfma_f32_32x32x16_bf16(a, b, c, 0, 0, 0)
#define MFMA16(a, b, c) __builtin_amdgcn_mfma_f32_16x16x32_bf16(a, b, c, 0, 0, 0)

constexpr int S_ = 2048, D_ = 576, F_ = 4608;
constexpr float EPS = 1e-5f;

DEV s16x4 vtr(lds_cptr p) { return __builtin_amdgcn_ds_read_tr16_b64_v4i16((lds_s16x4_ptr)p); }
DEV s16x8 cat8(s16x4 lo, s16x4 hi) { return __builtin_shufflevector(lo, hi, 0, 1, 2, 3, 4, 5, 6, 7); }
DEV unsigned pack_bf16(float a, float b) {
  bf2_t v; v[0] = (__bf16)a; v[1] = (__bf16)b; return __builtin_bit_cast(unsigned, v);
}
DEV float bf_lo(unsigned u) { return __builtin_bit_cast(float, u << 16); }
DEV float bf_hi(unsigned u) { return __builtin_bit_cast(float, u & 0xffff0000u); }
DEV float dot2bf(unsigned a, unsigned b, float c) {
  return __builtin_amdgcn_fdot2_f32_bf16(__builtin_bit_cast(bf2_t, a), __builtin_bit_cast(bf2_t, b), c, false);
}
DEV void glds16(unsigned voff, const void* sbase_, unsigned ldsaddr_) {
  unsigned sv;
  const uint64_t pv = (uint64_t)sbase_;
  const uint64_t sbase = ((uint64_t)(unsigned)__builtin_amdgcn_readfirstlane((int)(pv >> 32)) << 32) |
                         (uint64_t)(unsigned)__builtin_amdgcn_readfirstlane((int)(unsigned)pv);
  const unsigned ldsaddr = (unsigned)__builtin_amdgcn_readfirstlane((int)ldsaddr_);
  asm volatile("s_mov_b32 %0, m0\n\ts_mov_b32 m0, %3\n\ts_nop 0\n\tglobal_load_lds_dwordx4 %1, %2\n\ts_mov_b32 m0, %0"
               : "=&s"(sv) : "v"(voff), "s"(sbase), "s"(ldsaddr) : "memory");
}
DEV void gtouch(unsigned voff, const void* sbase_, unsigned ldsaddr_) {
  unsigned sv;
  const uint64_t pv = (uint64_t)sbase_;
  const uint64_t sbase = ((uint64_t)(unsigned)__builtin_amdgcn_readfirstlane((int)(pv >> 32)) << 32) |
                         (uint64_t)(unsigned)__builtin_amdgcn_readfirstlane((int)(unsigned)pv);
  const unsigned ldsaddr = (unsigned)__builtin_amdgcn_readfirstlane((int)ldsaddr_);
  asm volatile("s_mov_b32 %0, m0\n\ts_mov_b32 m0, %3\n\ts_nop 0\n\tglobal_load_lds_dword %1, %2\n\ts_mov_b32 m0, %0"
               : "=&s"(sv) : "v"(voff), "s"(sbase), "s"(ldsaddr) : "memory");
}
#define WAIT_BAR(N) asm volatile("s_waitcnt vmcnt(" #N ") lgkmcnt(0)\n\ts_barrier" ::: "memory")

template <int VM, int LGKM = 15> DEV void waitcnt() {
  __builtin_amdgcn_s_waitcnt((VM & 15) | ((VM >> 4) << 14) | (7 << 4) | ((LGKM & 15) << 8));
}
DEV void barrier() { asm volatile("s_barrier" ::: "memory"); }
template <int N> DEV void wait_bar() { asm volatile("s_waitcnt vmcnt(%0) lgkmcnt(0)\n\ts_barrier" ::"i"(N) : "memory"); }
template <int B, int E, class F> DEV void static_for(F&& f) {
  if constexpr (B < E) { f(std::integral_constant<int, B>{}); static_for<B + 1, E>(f); }
}
#define AINL __attribute__((always_inline))
#define ST_AUX 16
DEV void st_part(u32x4* base, unsigned off, u32x4 v) {
  __amdgpu_buffer_rsrc_t r = __builtin_amdgcn_make_buffer_rsrc((void*)base, 0, 16 * 147456 * 16, 0x00020000);
  __builtin_amdgcn_raw_buffer_store_b128(v, r, (int)off, 0, ST_AUX);
}

#define FS(i) do { } while (0)
#define STAMP(k, i) do { } while (0)
DEV float gelu_tanh(float x) {
  const float c1 = -2.0f * 1.4426950408889634f * 0.7978845608028654f;
  const float c3 = c1 * 0.044715f;
  float x2 = x * x;
  float t = x * __builtin_fmaf(x2, c3, c1);
  float e = __builtin_amdgcn_exp2f(t);
  return x * __builtin_amdgcn_rcpf(1.0f + e);
}

constexpr int K1_ROW = 288;
constexpr int K1_XO = 64 * K1_ROW;
constexpr int K1_SLOT = K1_XO + 144 * 128;
constexpr int K1_SCR = 4 * K1_SLOT;
constexpr int K1_LDS = K1_SCR + 256;
constexpr int K1_QS = 232;

__global__ __launch_bounds__(512, 2) void k1_qkv_attn(const u16* __restrict__ x, const u16* __restrict__ wqkv,
                                                      u16* __restrict__ o, const u16* __restrict__ wo,
                                                      const u16* __restrict__ w1, const u16* __restrict__ w2) {
  extern __shared__ __attribute__((aligned(16))) char smem[];
  const int tid = threadIdx.x, lane = tid & 63;
  const int wid8 = __builtin_amdgcn_readfirstlane(tid >> 6);
  const int wid = wid8 & 3;
  const int b = blockIdx.x, xcd = b & 7, bi = b >> 3;
  const int hp = 2 * xcd + (bi & 1), pnl = bi >> 1;
  const int par = hp & 1;
  const int i16 = lane & 15, g = lane >> 4, q4 = i16 >> 2, p4 = i16 & 3;
  const unsigned lds0 = (unsigned)(uintptr_t)smem;
  const int T0 = pnl * 128 + wid * 32;
  STAMP(0, 0);

  if (wid8 >= 4) {
  unsigned wo_[5], xo_[5];
  {
    const int segbase = (72 * hp) & ~15;
#pragma unroll
    for (int j = 0; j < 5; ++j) {
      const int n = j < 4 ? wid + 4 * j : 16 + (wid & 1);
      const int idx = n * 64 + lane;
      const int prow = idx / 18; int ch = idx - prow * 18; ch = ch > 14 ? 14 : ch;
      const int q = (prow >> 2) & 3;
      const int row = (prow & ~15) | ((q == 1 ? 2 : (q == 2 ? 1 : q)) << 2) | (prow & 3);
      const int seg = ch / 5, cc = ch - seg * 5;
      wo_[j] = (unsigned)(row * 3456 + seg * 1152 + segbase + cc * 16);
      const int xrow = idx >> 3, xch = idx & 7;
      int tok = pnl * 128 - 16 + xrow; tok = tok < 0 ? 0 : tok;
      xo_[j] = (unsigned)(tok * 1152 + ((xch ^ (xrow & 7)) * 16));
    }
  }
  auto issue_stage = [&](int st) AINL {
    const unsigned dst = lds0 + (unsigned)((st & 3) * K1_SLOT);
    const char* wb = (const char*)wqkv + (size_t)st * 64 * 3456;
    const char* xbp = (const char*)x + st * 128;
#pragma unroll
    for (int j = 0; j < 4; ++j) {
      glds16(wo_[j], wb, dst + (unsigned)((wid + 4 * j) * 1024));
      glds16(xo_[j], xbp, dst + (unsigned)(K1_XO + (wid + 4 * j) * 1024));
    }
    if (wid < 2) glds16(wo_[4], wb, dst + (unsigned)((16 + wid) * 1024));
    else glds16(xo_[4], xbp, dst + (unsigned)(K1_XO + (16 + wid - 2) * 1024));
  };
    issue_stage(0); issue_stage(1); issue_stage(2); issue_stage(3);
    waitcnt<27>(); barrier();
#pragma unroll 1
    for (int st = 0; st < 9; ++st) {
      if (st < 6) waitcnt<18>(); else if (st == 6) waitcnt<9>(); else waitcnt<0>();
      barrier();
      if (st + 4 < 9) issue_stage(st + 4);
    }
    barrier();
    if (bi < 21) {
      int L = bi * 256 + (tid & 255); L = L > 5183 ? 5183 : L;
      const int row = L / 9, l = L - row * 9;
      const unsigned scr = lds0 + (unsigned)K1_SCR;
      gtouch((unsigned)(row * 9216 + 1152 * xcd + l * 128), w1, scr);
      gtouch((unsigned)(663552 * xcd + L * 128), w2, scr);
      gtouch((unsigned)(L * 128), wo, scr);
    }
    return;
  }
  STAMP(0, 1);

  f32x4 acc[7][3];
#pragma unroll
  for (int ft = 0; ft < 7; ++ft)
#pragma unroll
    for (int ct = 0; ct < 3; ++ct) acc[ft][ct] = f32x4{0.f, 0.f, 0.f, 0.f};
  int aoff[7];
#pragma unroll
  for (int ft = 0; ft < 7; ++ft) {
    int G = 4 * ft + p4; G = G > 26 ? 26 : G;
    const int seg = G / 9, wi = G - seg * 9;
    aoff[ft] = (16 * (g >> 1) + 4 * (g & 1) + q4) * K1_ROW + seg * 80 + par * 8 + wi * 8;
  }
  int boff[3];
#pragma unroll
  for (int ct = 0; ct < 3; ++ct) {
    const int row = wid * 32 + ct * 16 + i16;
    boff[ct] = K1_XO + row * 128 + ((g ^ (row & 7)) * 16);
  }
  f32x4 asum[3], asq[3];
#pragma unroll
  for (int ct = 0; ct < 3; ++ct) { asum[ct] = f32x4{0.f, 0.f, 0.f, 0.f}; asq[ct] = f32x4{0.f, 0.f, 0.f, 0.f}; }
  const s16x8 ones8 = {0x3f80, 0x3f80, 0x3f80, 0x3f80, 0x3f80, 0x3f80, 0x3f80, 0x3f80};
  s16x8 xbA[3], xbB[3];
  s16x4 afA[14], afB[14];
  auto load_job = [&](auto J_, auto KS_, int st, s16x8 (&xb)[3], s16x4 (&af)[14]) AINL {
    constexpr int j = decltype(J_)::value, ks = decltype(KS_)::value;
    lds_cptr sl = (lds_cptr)smem + (st & 3) * K1_SLOT;
    if constexpr (j < 3) xb[j] = *(__attribute__((address_space(3))) const s16x8*)(sl + (boff[j] ^ (ks * 64)));
    else af[j - 3] = vtr(sl + aoff[(j - 3) >> 1] + ks * 32 * K1_ROW + ((j - 3) & 1) * 8 * K1_ROW);
  };
  auto half = [&](auto KS_, int st, s16x8 (&xc)[3], s16x4 (&ac)[14], s16x8 (&xn)[3], s16x4 (&an)[14]) AINL {
    constexpr int ks = decltype(KS_)::value;
    if constexpr (ks == 1) {
      FS(3 * st + 1);
      waitcnt<63, 0>();
      barrier();
      FS(3 * st + 2);
    } else { FS(3 * st); }
    s16x8 xh;
    static_for<0, 27>([&](auto M_) AINL {
      constexpr int r = M_;
      if constexpr (r < 3) asq[r] = MFMA16(xc[r], xc[r], asq[r]);
      else if constexpr (r < 6) asum[r - 3] = MFMA16(ones8, xc[r - 3], asum[r - 3]);
      else {
        constexpr int ft = (r - 6) / 3, ct = (r - 6) % 3;
        acc[ft][ct] = MFMA16(cat8(ac[2 * ft], ac[2 * ft + 1]), ct == 0 ? xh : xc[ct], acc[ft][ct]);
      }
      if constexpr (r == 4) { xh = xc[0]; if (i16 == 0) xh = ones8; }
      {
        constexpr int u = r;
        if constexpr (u < 17) load_job(std::integral_constant<int, u>{}, std::integral_constant<int, 1 - ks>{}, st + ks, xn, an);
      }
      __builtin_amdgcn_sched_barrier(0);
    });
  };
  using KS0 = std::integral_constant<int, 0>; using KS1 = std::integral_constant<int, 1>;
  barrier();
  STAMP(0, 6);
  static_for<0, 17>([&](auto J_) AINL { load_job(J_, KS0{}, 0, xbA, afA); });
#pragma unroll 1
  for (int st = 0; st < 9; ++st) {
    half(KS0{}, st, xbA, afA, xbB, afB);
    half(KS1{}, st, xbB, afB, xbA, afA);
  }
  float ssum[3], ssq[3];
#pragma unroll
  for (int ct = 0; ct < 3; ++ct) {
    ssum[ct] = asum[ct][0];
    float dsel = (i16 & 2) ? ((i16 & 1) ? asq[ct][3] : asq[ct][2]) : ((i16 & 1) ? asq[ct][1] : asq[ct][0]);
    ssq[ct] = __shfl(dsel, (i16 >> 2) * 16 + i16);
  }
  STAMP(0, 2);
  WAIT_BAR(0);
  lds_ptr qs = (lds_ptr)smem + wid * (48 * K1_QS);
#pragma unroll
  for (int ct = 0; ct < 3; ++ct) {
    const float mean = ssum[ct] * (1.0f / 576.0f);
    float var = ssq[ct] * (1.0f / 576.0f) - mean * mean;
    var = var < 0.f ? 0.f : var;
    const float rs = 1.0f / sqrtf(var + EPS);
    const float sm = rs * mean;
#pragma unroll
    for (int ft = 0; ft < 7; ++ft) {
      const int G = 4 * ft + g;
      float v[4];
#pragma unroll
      for (int r = 0; r < 4; ++r) {
        const float c = __shfl(acc[ft][0][r], lane & 48);
        v[r] = rs * acc[ft][ct][r] - sm * c;
      }
      if (G < 27) {
        u32x2 pk = {pack_bf16(v[0], v[1]), pack_bf16(v[2], v[3])};
        *(__attribute__((address_space(3))) u32x2*)(qs + (ct * 16 + i16) * K1_QS + G * 8) = pk;
      }
    }
  }
  asm volatile("s_waitcnt lgkmcnt(0)" ::: "memory");
  STAMP(0, 3);
  {
    const int tk = lane & 31, hd = lane >> 5;
    lds_cptr qrow = (lds_cptr)qs + (16 + tk) * K1_QS + hd * 36;
    unsigned qv[9];
#pragma unroll
    for (int d = 0; d < 9; ++d) qv[d] = *(__attribute__((address_space(3))) const unsigned*)(qrow + d * 4);
    float lg[8];
#pragma unroll
    for (int w = 0; w < 8; ++w) {
      lds_cptr krow = qrow - 2 * w * K1_QS + 72;
      float a = 0.f;
#pragma unroll
      for (int d = 0; d < 9; ++d) a = dot2bf(qv[d], *(__attribute__((address_space(3))) const unsigned*)(krow + d * 4), a);
      lg[w] = (T0 + tk - 2 * w >= 0) ? a * 0.23570226039551584f : -1e9f;
    }
    float mx = lg[0];
#pragma unroll
    for (int w = 1; w < 8; ++w) mx = fmaxf(mx, lg[w]);
    float se = 0.f;
#pragma unroll
    for (int w = 0; w < 8; ++w) { lg[w] = __builtin_amdgcn_exp2f((lg[w] - mx) * 1.4426950408889634f); se += lg[w]; }
    const float inv = 1.0f / se;
    float ov[18];
#pragma unroll
    for (int d = 0; d < 18; ++d) ov[d] = 0.f;
#pragma unroll
    for (int w = 0; w < 8; ++w) {
      lds_cptr vrow = qrow - 2 * w * K1_QS + 144;
      const float pw = lg[w] * inv;
#pragma unroll
      for (int d = 0; d < 9; ++d) {
        const unsigned u = *(__attribute__((address_space(3))) const unsigned*)(vrow + d * 4);
        ov[2 * d] = __builtin_fmaf(pw, bf_lo(u), ov[2 * d]);
        ov[2 * d + 1] = __builtin_fmaf(pw, bf_hi(u), ov[2 * d + 1]);
      }
    }
    {
      const int tok = T0 + tk, c0 = hp * 36 + hd * 18;
      char* ob_ = (char*)o + (size_t)((tok >> 4) * 18 * 64 + (tok & 15)) * 16;
#pragma unroll
      for (int d = 0; d < 9; ++d) {
        const int col = c0 + 2 * d;
        *(unsigned*)(ob_ + (col >> 3) * 256 + (col & 7) * 2) = pack_bf16(ov[2 * d], ov[2 * d + 1]);
      }
    }
  }
  STAMP(0, 4); STAMP(0, 5);
}

constexpr int K2_LDS = 56 * 1024;

__global__ __launch_bounds__(256, 1) void k2_proj(const u16* __restrict__ o, const u16* __restrict__ x,
                                                  const u16* __restrict__ wo, const u16* __restrict__ w1,
                                                  u16* __restrict__ h, float* __restrict__ stats, float* __restrict__ c1) {
  extern __shared__ __attribute__((aligned(16))) char smem[];
  const int tid = threadIdx.x, lane = tid & 63;
  const int b = blockIdx.x;
  if (b >= 192) {
    const int cb = b - 192;
    const int cg = tid % 9, rg = tid / 9;
    float* red = (float*)smem;
    if (rg < 24) {
      u32x4 v[24];
#pragma unroll
      for (int i = 0; i < 24; ++i) v[i] = *(const u32x4*)(w1 + (size_t)(rg * 24 + i) * F_ + cb * 72 + cg * 8);
      float s[8];
#pragma unroll
      for (int e = 0; e < 8; ++e) s[e] = 0.f;
#pragma unroll
      for (int i = 0; i < 24; ++i)
#pragma unroll
        for (int e = 0; e < 4; ++e) { s[2 * e] += bf_lo(v[i][e]); s[2 * e + 1] += bf_hi(v[i][e]); }
#pragma unroll
      for (int e = 0; e < 8; ++e) red[rg * 72 + cg * 8 + e] = s[e];
    }
    __syncthreads();
    if (tid < 72) {
      float s = 0.f;
      for (int r = 0; r < 24; ++r) s += red[r * 72 + tid];
      c1[cb * 72 + tid] = s;
    }
    return;
  }
  const int wid = __builtin_amdgcn_readfirstlane(tid >> 6);
  STAMP(1, 0);
  const int xcd = b & 7, bi = b >> 3;
  const int pnl = 2 * xcd + (bi >= 12 ? 1 : 0), n = bi >= 12 ? bi - 12 : bi;
  const int i16 = lane & 15, g = lane >> 4, q4 = i16 >> 2, p4 = i16 & 3;
  const unsigned lds0 = (unsigned)(uintptr_t)smem;
  const int T0 = pnl * 128 + wid * 32;

  s16x8 ob[2][18];
#pragma unroll
  for (int ct = 0; ct < 2; ++ct) {
    const u32x4* op_ = (const u32x4*)o + (size_t)((T0 >> 4) + ct) * 18 * 64 + lane;
#pragma unroll
    for (int ks = 0; ks < 18; ++ks) ob[ct][ks] = __builtin_bit_cast(s16x8, op_[ks * 64]);
  }
#pragma unroll
  for (int j = 0; j < 14; ++j) {
    int ni = j * 4 + wid; ni = ni > 53 ? 53 : ni;
    const int idx = ni * 64 + lane;
    const int row = idx / 6, ch = idx - row * 6;
    glds16((unsigned)(row * 1152 + n * 96 + ch * 16), wo, lds0 + (unsigned)(ni * 1024));
  }
  u32x2 xr[2][3];
#pragma unroll
  for (int ct = 0; ct < 2; ++ct)
#pragma unroll
    for (int ft = 0; ft < 3; ++ft)
      xr[ct][ft] = *(const u32x2*)(x + (size_t)(T0 + ct * 16 + i16) * D_ + n * 48 + ft * 16 + 4 * g);

  f32x4 acc[3][2];
#pragma unroll
  for (int ft = 0; ft < 3; ++ft)
#pragma unroll
    for (int ct = 0; ct < 2; ++ct) acc[ft][ct] = f32x4{0.f, 0.f, 0.f, 0.f};
  lds_cptr abase = (lds_cptr)smem + (8 * g + q4) * 96 + p4 * 8;
  STAMP(1, 1);
  WAIT_BAR(0);
  STAMP(1, 2);
#pragma unroll
  for (int ks = 0; ks < 18; ++ks)
#pragma unroll
    for (int ft = 0; ft < 3; ++ft) {
      s16x8 a = cat8(vtr(abase + ks * 32 * 96 + ft * 32), vtr(abase + ks * 32 * 96 + ft * 32 + 4 * 96));
#pragma unroll
      for (int ct = 0; ct < 2; ++ct) acc[ft][ct] = MFMA16(a, ob[ct][ks], acc[ft][ct]);
    }
#pragma unroll
  for (int ct = 0; ct < 2; ++ct) {
    const int tok = T0 + ct * 16 + i16;
    float hv[12];
#pragma unroll
    for (int ft = 0; ft < 3; ++ft) {
      const unsigned p0 = pack_bf16(bf_lo(xr[ct][ft][0]) + acc[ft][ct][0], bf_hi(xr[ct][ft][0]) + acc[ft][ct][1]);
      const unsigned p1 = pack_bf16(bf_lo(xr[ct][ft][1]) + acc[ft][ct][2], bf_hi(xr[ct][ft][1]) + acc[ft][ct][3]);
      u32x2 pk = {p0, p1};
      {
        const int col = n * 48 + ft * 16 + 4 * g;
        const int ks = col >> 4, hh2 = (col >> 3) & 1, j4 = col & 4;
        *(u32x2*)(h + ((size_t)(((tok >> 5) * 36 + ks) * 64 + hh2 * 32 + (tok & 31)) * 8 + j4)) = pk;
      }
      hv[ft * 4 + 0] = bf_lo(p0); hv[ft * 4 + 1] = bf_hi(p0); hv[ft * 4 + 2] = bf_lo(p1); hv[ft * 4 + 3] = bf_hi(p1);
    }
    float s = 0.f;
#pragma unroll
    for (int e = 0; e < 12; ++e) s += hv[e];
    s += __shfl_xor(s, 16); s += __shfl_xor(s, 32);
    const float mean = s * (1.0f / 48.0f);
    float m2 = 0.f;
#pragma unroll
    for (int e = 0; e < 12; ++e) { const float d = hv[e] - mean; m2 = __builtin_fmaf(d, d, m2); }
    m2 += __shfl_xor(m2, 16); m2 += __shfl_xor(m2, 32);
    if (g == 0) { stats[(size_t)tok * 24 + n * 2] = s; stats[(size_t)tok * 24 + n * 2 + 1] = m2; }
  }
  STAMP(1, 3); STAMP(1, 4);
}

constexpr int HS = 18432;
constexpr int K3_XR = 4 * HS;
constexpr int K3_TAB = K3_XR + 73728;
constexpr int K3_SCR = K3_TAB + 288 * 8;
constexpr int K3_LDS = K3_SCR + 256;

__global__ __launch_bounds__(512, 2) void k3_mlp(const u32x4* __restrict__ hT, const float* __restrict__ stats,
                                                 const float* __restrict__ c1, const u16* __restrict__ w1,
                                                 const u16* __restrict__ b1, const u16* __restrict__ w2,
                                                 u32x4* __restrict__ part) {
  extern __shared__ __attribute__((aligned(16))) char smem[];
  const int tid = threadIdx.x, lane = tid & 63;
  const int wid = __builtin_amdgcn_readfirstlane(tid >> 6);
  const int b = blockIdx.x, xcd = b & 7, bi = b >> 3;
  const int c = 2 * xcd + (bi & 1), pnl = bi >> 1;
  const unsigned lds0 = (unsigned)(uintptr_t)smem;
  STAMP(2, 0);

  if (wid >= 4) {
    const int lw = wid - 4;
    unsigned o1[5], o2[5];
#pragma unroll
    for (int j = 0; j < 5; ++j) {
      const int n = j < 4 ? lw + 4 * j : 16 + (lw & 1);
      const int row = n * 16 + (lane >> 2), ch = lane & 3;
      o1[j] = (unsigned)(row * (F_ * 2) + ch * 16);
      o2[j] = (unsigned)(row * (D_ * 2) + ch * 16);
    }
    const char* w1c = (const char*)w1 + (size_t)(288 * c) * 2;
    const char* w2c = (const char*)w2 + (size_t)(288 * c) * D_ * 2;
    auto issue_step = [&](int s) AINL {
      const bool is2 = s >= 18;
      const char* base = is2 ? w2c + (size_t)(64 * (s - 18)) : w1c + ((size_t)(288 * (s & 1)) * F_ + 32 * (s >> 1)) * 2;
      const unsigned dst = lds0 + (unsigned)((s & 3) * HS);
#pragma unroll
      for (int j = 0; j < 4; ++j) glds16(is2 ? o2[j] : o1[j], base, dst + (unsigned)((lw + 4 * j) * 1024));
      if ((lw >> 1) == (s & 1)) glds16(is2 ? o2[4] : o1[4], base, dst + (unsigned)((16 + (lw & 1)) * 1024));
    };
    issue_step(0); issue_step(1); issue_step(2);
    {
      float* tc = (float*)(smem + K3_TAB);
      const int t = tid - 256;
      tc[t] = c1[288 * c + t]; tc[288 + t] = bf_lo(b1[288 * c + t]);
      if (t < 32) { tc[256 + t] = c1[288 * c + 256 + t]; tc[288 + 256 + t] = bf_lo(b1[288 * c + 256 + t]); }
    }
    waitcnt<4, 0>(); barrier();
#pragma unroll 1
    for (int s = 0; s < 36; ++s) {
      if (s + 3 < 36) issue_step(s + 3);
      if (s + 3 < 36) waitcnt<4>(); else waitcnt<0>();
      barrier();
    }
    return;
  }

  const int t32 = lane & 31, hh = lane >> 5;
  const int tok = pnl * 128 + wid * 32 + t32;
  const int i16 = lane & 15, q4 = i16 >> 2, p4 = i16 & 3, blk = (lane >> 4) & 1;
  s16x8 hb[36];
  {
    const u32x4* hp = hT + (size_t)((pnl * 4 + wid) * 36) * 64 + lane;
#pragma unroll
    for (int ks = 0; ks < 36; ++ks) hb[ks] = __builtin_bit_cast(s16x8, hp[ks * 64]);
  }
  float sc, scm;
  {
    f32x4 stv[6];
#pragma unroll
    for (int i = 0; i < 6; ++i) stv[i] = *(const f32x4*)(stats + (size_t)tok * 24 + i * 4);
    float sum = 0.f;
#pragma unroll
    for (int i = 0; i < 6; ++i) sum += stv[i][0] + stv[i][2];
    const float mean = sum * (1.0f / 576.0f);
    float m2 = 0.f;
#pragma unroll
    for (int i = 0; i < 6; ++i) {
      const float d0 = stv[i][0] * (1.0f / 48.0f) - mean, d1 = stv[i][2] * (1.0f / 48.0f) - mean;
      m2 += stv[i][1] + stv[i][3] + 48.0f * (d0 * d0 + d1 * d1);
    }
    const float var = m2 * (1.0f / 576.0f);
    const float r1 = __builtin_amdgcn_rsqf(var + EPS);
    const float var2 = var * r1 * r1;
    const float r2 = __builtin_amdgcn_rsqf(var2 + EPS);
    sc = r1 * r2; scm = sc * mean;
  }
  const int lch = (16 * blk + 4 * p4) * 2;
  lds_cptr lb1 = (lds_cptr)smem + (8 * hh + q4) * 64 + lch;
  lds_cptr lb2 = (lds_cptr)smem + (4 * hh + q4) * 64 + lch;
  lds_ptr xw = (lds_ptr)smem + K3_XR + wid * 18432 + lane * 16;

  constexpr int LA = 4;
  s16x8 fr[LA];
  f32x16 accA, accB, zero16;
#pragma unroll
  for (int r = 0; r < 16; ++r) { accA[r] = 0.f; accB[r] = 0.f; zero16[r] = 0.f; }
  using TT = std::true_type; using FF = std::false_type;
  using P0 = std::integral_constant<int, 0>; using P2 = std::integral_constant<int, 2>;

  STAMP(2, 1);
  waitcnt<0, 0>(); barrier();
  STAMP(2, 2);
  static_for<0, LA>([&](auto M) AINL {
    constexpr int m = M;
    lds_cptr a = lb1 + m * 16 * 64;
    fr[m] = cat8(vtr(a), vtr(a + 4 * 64));
  });

  auto pass1 = [&](auto LAST_, f32x16& acc, const f32x16& accp, int it) AINL {
    constexpr bool LAST = decltype(LAST_)::value;
    const int ftp = it == 0 ? 0 : it - 1;
    const int oA = ((2 * it) & 3) * HS, oN = ((2 * it + 2) & 3) * HS;
    unsigned gp[8];
    float v0 = 0.f, v1 = 0.f, u0 = 0.f, u1 = 0.f;
    f32x4 tcc, tbb;
    static_for<0, 36>([&](auto MM) AINL {
      constexpr int m = MM;
      if constexpr (m == 18) barrier();
      acc = MFMA32(fr[m % LA], hb[m], m == 0 ? zero16 : acc);
      constexpr int mn = m + LA;
      lds_cptr a = mn < 36 ? lb1 + oA + (mn / 18) * HS + (mn % 18) * 16 * 64 : (LAST ? lb2 : lb1) + oN + (mn - 36) * 16 * 64;
      constexpr int r2 = (mn >= 36 && LAST) ? 8 * 64 : 4 * 64;
      s16x4 flo, fhi;
      constexpr int ph = (m - 4) & 3, e = ((m - 4) >> 2) * 2;
      const float gc1 = -2.0f * 1.4426950408889634f * 0.7978845608028654f, gc3 = gc1 * 0.044715f;
      if constexpr (m >= 4 && ph == 0) {
        u0 = __builtin_fmaf(-scm, tcc[e & 3], tbb[e & 3]); u1 = __builtin_fmaf(-scm, tcc[(e & 3) + 1], tbb[(e & 3) + 1]);
        flo = vtr(a);
        v0 = __builtin_fmaf(sc, accp[e], u0); v1 = __builtin_fmaf(sc, accp[e + 1], u1);
        fhi = vtr(a + r2);
        u0 = v0 * v0;
      } else if constexpr (m >= 4 && ph == 1) {
        u1 = v1 * v1; u0 = __builtin_fmaf(u0, gc3, gc1);
        flo = vtr(a);
        u1 = __builtin_fmaf(u1, gc3, gc1); u0 = v0 * u0;
        fhi = vtr(a + r2);
        u1 = v1 * u1;
      } else if constexpr (m >= 4 && ph == 2) {
        u0 = __builtin_amdgcn_exp2f(u0); u1 = __builtin_amdgcn_exp2f(u1);
        flo = vtr(a); fhi = vtr(a + r2);
        u0 = 1.0f + u0; u1 = 1.0f + u1;
      } else if constexpr (m >= 4 && ph == 3) {
        u0 = __builtin_amdgcn_rcpf(u0); u1 = __builtin_amdgcn_rcpf(u1);
        flo = vtr(a); fhi = vtr(a + r2);
        gp[e >> 1] = pack_bf16(v0 * u0, v1 * u1);
        asm volatile("" : "+v"(gp[e >> 1]));
      } else { flo = vtr(a); fhi = vtr(a + r2); }
      asm volatile("" : "+v"(v0), "+v"(v1), "+v"(u0), "+v"(u1));
      if constexpr ((m & 7) == 1 && m < 32) {
        lds_cptr tc = (lds_cptr)smem + K3_TAB + (32 * ftp + 4 * hh + m - 1) * 4;
        tcc = *(__attribute__((address_space(3))) const f32x4*)tc;
        tbb = *(__attribute__((address_space(3))) const f32x4*)(tc + 288 * 4);
      }
      if constexpr (m == 20) *(__attribute__((address_space(3))) u32x4*)(xw + (ftp * 2) * 1024) = u32x4{gp[0], gp[1], gp[2], gp[3]};
      fr[m % LA] = cat8(flo, fhi);
      __builtin_amdgcn_sched_barrier(0);
    });
    *(__attribute__((address_space(3))) u32x4*)(xw + (ftp * 2 + 1) * 1024) = u32x4{gp[4], gp[5], gp[6], gp[7]};
    barrier();
  };
#pragma unroll 1
  for (int i2 = 0; i2 < 4; ++i2) {
    pass1(FF{}, accA, accB, 2 * i2);
    pass1(FF{}, accB, accA, 2 * i2 + 1);
  }
  pass1(TT{}, accA, accB, 8);
  STAMP(2, 3);
  {
    float gv[16];
#pragma unroll
    for (int rg = 0; rg < 4; ++rg) {
      lds_cptr tc = (lds_cptr)smem + K3_TAB + (32 * 8 + 4 * hh + 8 * rg) * 4;
      const f32x4 cc = *(__attribute__((address_space(3))) const f32x4*)tc;
      const f32x4 bb = *(__attribute__((address_space(3))) const f32x4*)(tc + 288 * 4);
#pragma unroll
      for (int e = 0; e < 4; ++e) gv[rg * 4 + e] = gelu_tanh(__builtin_fmaf(sc, accA[rg * 4 + e], __builtin_fmaf(-scm, cc[e], bb[e])));
    }
#pragma unroll
    for (int s = 0; s < 2; ++s) {
      u32x4 pk = {pack_bf16(gv[8 * s], gv[8 * s + 1]), pack_bf16(gv[8 * s + 2], gv[8 * s + 3]),
                  pack_bf16(gv[8 * s + 4], gv[8 * s + 5]), pack_bf16(gv[8 * s + 6], gv[8 * s + 7])};
      *(__attribute__((address_space(3))) u32x4*)(xw + (16 + s) * 1024) = pk;
    }
  }
  s16x8 xbf[18];
#pragma unroll
  for (int k = 0; k < 18; ++k) xbf[k] = *(__attribute__((address_space(3))) const s16x8*)(xw + k * 1024);
  STAMP(2, 4);

  const unsigned doff = (unsigned)((((c * 16 + pnl) * 4 + wid) * 36) * 64 + lane) * 16u;
  auto pass2 = [&](auto LAST_, auto PH_, f32x16& acc, const f32x16& accp, int d) AINL {
    constexpr bool LAST = decltype(LAST_)::value;
    constexpr int PH = decltype(PH_)::value;
    const int oA = ((18 + d) & 3) * HS, oN = ((19 + d) & 3) * HS;
    u32x4 pk0, pk1;
    static_for<0, 18>([&](auto MM) AINL {
      constexpr int m = MM;
      acc = MFMA32(fr[(m + PH) % LA], xbf[m], m == 0 ? zero16 : acc);
      constexpr int mn = m + LA;
      if constexpr (mn < 18) { lds_cptr a = lb2 + oA + mn * 16 * 64; fr[(m + PH) % LA] = cat8(vtr(a), vtr(a + 8 * 64)); }
      else if constexpr (!LAST) { lds_cptr a = lb2 + oN + (mn - 18) * 16 * 64; fr[(m + PH) % LA] = cat8(vtr(a), vtr(a + 8 * 64)); }
      if constexpr (m >= 2 && m < 6) { pk0[m - 2] = pack_bf16(accp[2 * (m - 2)], accp[2 * (m - 2) + 1]); pk1[m - 2] = pack_bf16(accp[8 + 2 * (m - 2)], accp[8 + 2 * (m - 2) + 1]); }
      if constexpr (m == 6) { if (d > 0) st_part(part, doff + (unsigned)(d - 1) * 2048u, pk0); }
      if constexpr (m == 8) { if (d > 0) st_part(part, doff + (unsigned)(d - 1) * 2048u + 1024u, pk1); }
      __builtin_amdgcn_sched_barrier(0);
    });
    if constexpr (!LAST) barrier();
  };
#pragma unroll 1
  for (int d2 = 0; d2 < 8; ++d2) {
    pass2(FF{}, P0{}, accA, accB, 2 * d2);
    pass2(FF{}, P2{}, accB, accA, 2 * d2 + 1);
  }
  pass2(FF{}, P0{}, accA, accB, 16);
  pass2(TT{}, P2{}, accB, accA, 17);
  barrier();
#pragma unroll
  for (int h2 = 0; h2 < 2; ++h2) {
    u32x4 pk = {pack_bf16(accB[8 * h2], accB[8 * h2 + 1]), pack_bf16(accB[8 * h2 + 2], accB[8 * h2 + 3]),
                pack_bf16(accB[8 * h2 + 4], accB[8 * h2 + 5]), pack_bf16(accB[8 * h2 + 6], accB[8 * h2 + 7])};
    st_part(part, (unsigned)((((c * 16 + pnl) * 4 + wid) * 36 + 17 * 2 + h2) * 64 + lane) * 16u, pk);
  }
  STAMP(2, 5);
}

__global__ __launch_bounds__(256) void k4_reduce(const u32x4* __restrict__ part, const u16* __restrict__ h,
                                                 const u16* __restrict__ b2, u16* __restrict__ out) {
  const int gid = blockIdx.x * 256 + threadIdx.x;
  const int lane = gid & 63, rest = gid >> 6;
  const int h2 = rest & 1, dtl = (rest >> 1) & 1, r1 = rest >> 2;
  const int dp = r1 % 9, r3 = r1 / 9;
  const int t32 = lane & 31, hh = lane >> 5;
  const int tok = r3 * 32 + t32;
  const int d0 = 64 * dp + 32 * dtl + 16 * h2 + 4 * hh;
  u32x4 v[16];
#pragma unroll
  for (int c = 0; c < 16; ++c) v[c] = part[(size_t)c * 147456 + gid];
  const u16* hp = h + ((size_t)((r3 * 36 + (d0 >> 4)) * 64 + t32) * 8 + 4 * hh);
  const u32x2 ha = *(const u32x2*)hp, hb = *(const u32x2*)(hp + 256);
  const u32x2 ba = *(const u32x2*)(b2 + d0), bb = *(const u32x2*)(b2 + d0 + 8);
  float s[8];
  s[0] = bf_lo(ha[0]) + bf_lo(ba[0]); s[1] = bf_hi(ha[0]) + bf_hi(ba[0]);
  s[2] = bf_lo(ha[1]) + bf_lo(ba[1]); s[3] = bf_hi(ha[1]) + bf_hi(ba[1]);
  s[4] = bf_lo(hb[0]) + bf_lo(bb[0]); s[5] = bf_hi(hb[0]) + bf_hi(bb[0]);
  s[6] = bf_lo(hb[1]) + bf_lo(bb[1]); s[7] = bf_hi(hb[1]) + bf_hi(bb[1]);
  float a[8];
#pragma unroll
  for (int e = 0; e < 8; ++e) a[e] = 0.f;
#pragma unroll
  for (int c = 0; c < 16; ++c)
#pragma unroll
    for (int e = 0; e < 4; ++e) { a[2 * e] += bf_lo(v[c][e]); a[2 * e + 1] += bf_hi(v[c][e]); }
  u32x2 oa = {pack_bf16(s[0] + a[0], s[1] + a[1]), pack_bf16(s[2] + a[2], s[3] + a[3])};
  u32x2 ob = {pack_bf16(s[4] + a[4], s[5] + a[5]), pack_bf16(s[6] + a[6], s[7] + a[7])};
  *(u32x2*)(out + (size_t)tok * D_ + d0) = oa;
  *(u32x2*)(out + (size_t)tok * D_ + d0 + 8) = ob;
}

extern "C" void kernel_launch(void* const* d_in, const int* in_sizes, int n_in, void* d_out, int out_size,
                              void* d_ws, size_t ws_size, hipStream_t stream) {
  const u16* x = (const u16*)d_in[0];
  const u16* wqkv = (const u16*)d_in[1];
  const u16* wo = (const u16*)d_in[2];
  const u16* w1 = (const u16*)d_in[4];
  const u16* b1 = (const u16*)d_in[5];
  const u16* w2 = (const u16*)d_in[6];
  const u16* b2 = (const u16*)d_in[7];
  u16* out = (u16*)d_out;
  char* ws = (char*)d_ws;
  u16* o = (u16*)ws;
  u16* h = (u16*)(ws + 2359296);
  float* stats = (float*)(ws + 2 * 2359296);
  float* c1 = (float*)(ws + 2 * 2359296 + 196608);
  u32x4* part = (u32x4*)(ws + 2 * 2359296 + 196608 + 18432 + 2048);

  hipFuncSetAttribute((const void*)k1_qkv_attn, hipFuncAttributeMaxDynamicSharedMemorySize, K1_LDS);
  hipFuncSetAttribute((const void*)k2_proj, hipFuncAttributeMaxDynamicSharedMemorySize, K2_LDS);
  hipFuncSetAttribute((const void*)k3_mlp, hipFuncAttributeMaxDynamicSharedMemorySize, K3_LDS);

  k1_qkv_attn<<<256, 512, K1_LDS, stream>>>(x, wqkv, o, wo, w1, w2);
  k2_proj<<<256, 256, K2_LDS, stream>>>(o, x, wo, w1, h, stats, c1);
  k3_mlp<<<256, 512, K3_LDS, stream>>>((const u32x4*)h, stats, c1, w1, b1, w2, part);
  k4_reduce<<<576, 256, 0, stream>>>(part, h, b2, out);
}
```

```cpp
#include <hip/hip_runtime.h>
#include <hip/hip_bf16.h>
#include <stdint.h>
#include <type_traits>


typedef short s16x4 __attribute__((ext_vector_type(4)));
typedef short s16x8 __attribute__((ext_vector_type(8)));
typedef __bf16 bf2_t __attribute__((ext_vector_type(2)));
typedef float f32x4 __attribute__((ext_vector_type(4)));
typedef float f32x16 __attribute__((ext_vector_type(16)));
typedef unsigned u32x4 __attribute__((ext_vector_type(4)));
typedef unsigned u32x2 __attribute__((ext_vector_type(2)));
typedef unsigned short u16;
typedef __attribute__((address_space(3))) const char* lds_cptr;
typedef __attribute__((address_space(3))) char* lds_ptr;
typedef __attribute__((address_space(3))) s16x4* lds_s16x4_ptr;

#define DEV __device__ __forceinline__
#define MFMA32(a, b, c) __builtin_amdgcn_mfma_f32_32x32x16_bf16(a, b, c, 0, 0, 0)
#define MFMA16(a, b, c) __builtin_amdgcn_mfma_f32_16x16x32_bf16(a, b, c, 0, 0, 0)

constexpr int S_ = 2048, D_ = 576, F_ = 4608;
constexpr float EPS = 1e-5f;

DEV s16x4 vtr(lds_cptr p) { return __builtin_amdgcn_ds_read_tr16_b64_v4i16((lds_s16x4_ptr)p); }
DEV s16x8 cat8(s16x4 lo, s16x4 hi) { return __builtin_shufflevector(lo, hi, 0, 1, 2, 3, 4, 5, 6, 7); }
DEV unsigned pack_bf16(float a, float b) {
  bf2_t v; v[0] = (__bf16)a; v[1] = (__bf16)b; return __builtin_bit_cast(unsigned, v);
}
DEV float bf_lo(unsigned u) { return __builtin_bit_cast(float, u << 16); }
DEV float bf_hi(unsigned u) { return __builtin_bit_cast(float, u & 0xffff0000u); }
DEV float dot2bf(unsigned a, unsigned b, float c) {
  return __builtin_amdgcn_fdot2_f32_bf16(__builtin_bit_cast(bf2_t, a), __builtin_bit_cast(bf2_t, b), c, false);
}
DEV void glds16(unsigned voff, const void* sbase_, unsigned ldsaddr_) {
  unsigned sv;
  const uint64_t pv = (uint64_t)sbase_;
  const uint64_t sbase = ((uint64_t)(unsigned)__builtin_amdgcn_readfirstlane((int)(pv >> 32)) << 32) |
                         (uint64_t)(unsigned)__builtin_amdgcn_readfirstlane((int)(unsigned)pv);
  const unsigned ldsaddr = (unsigned)__builtin_amdgcn_readfirstlane((int)ldsaddr_);
  asm volatile("s_mov_b32 %0, m0\n\ts_mov_b32 m0, %3\n\ts_nop 0\n\tglobal_load_lds_dwordx4 %1, %2\n\ts_mov_b32 m0, %0"
               : "=&s"(sv) : "v"(voff), "s"(sbase), "s"(ldsaddr) : "memory");
}
DEV void gtouch(unsigned voff, const void* sbase_, unsigned ldsaddr_) {
  unsigned sv;
  const uint64_t pv = (uint64_t)sbase_;
  const uint64_t sbase = ((uint64_t)(unsigned)__builtin_amdgcn_readfirstlane((int)(pv >> 32)) << 32) |
                         (uint64_t)(unsigned)__builtin_amdgcn_readfirstlane((int)(unsigned)pv);
  const unsigned ldsaddr = (unsigned)__builtin_amdgcn_readfirstlane((int)ldsaddr_);
  asm volatile("s_mov_b32 %0, m0\n\ts_mov_b32 m0, %3\n\ts_nop 0\n\tglobal_load_lds_dword %1, %2\n\ts_mov_b32 m0, %0"
               : "=&s"(sv) : "v"(voff), "s"(sbase), "s"(ldsaddr) : "memory");
}
#define WAIT_BAR(N) asm volatile("s_waitcnt vmcnt(" #N ") lgkmcnt(0)\n\ts_barrier" ::: "memory")

template <int VM, int LGKM = 15> DEV void waitcnt() {
  __builtin_amdgcn_s_waitcnt((VM & 15) | ((VM >> 4) << 14) | (7 << 4) | ((LGKM & 15) << 8));
}
DEV void barrier() { asm volatile("s_barrier" ::: "memory"); }
template <int N> DEV void wait_bar() { asm volatile("s_waitcnt vmcnt(%0) lgkmcnt(0)\n\ts_barrier" ::"i"(N) : "memory"); }
template <int B, int E, class F> DEV void static_for(F&& f) {
  if constexpr (B < E) { f(std::integral_constant<int, B>{}); static_for<B + 1, E>(f); }
}
#define AINL __attribute__((always_inline))
#define ST_AUX 16
DEV void st_part(u32x4* base, unsigned off, u32x4 v) {
  __amdgpu_buffer_rsrc_t r = __builtin_amdgcn_make_buffer_rsrc((void*)base, 0, 16 * 147456 * 16, 0x00020000);
  __builtin_amdgcn_raw_buffer_store_b128(v, r, (int)off, 0, ST_AUX);
}

DEV float gelu_tanh(float x) {
  const float c1 = -2.0f * 1.4426950408889634f * 0.7978845608028654f;
  const float c3 = c1 * 0.044715f;
  float x2 = x * x;
  float t = x * __builtin_fmaf(x2, c3, c1);
  float e = __builtin_amdgcn_exp2f(t);
  return x * __builtin_amdgcn_rcpf(1.0f + e);
}

constexpr int K1_ROW = 288;
constexpr int K1_XO = 64 * K1_ROW;
constexpr int K1_SLOT = K1_XO + 144 * 128;
constexpr int K1_SCR = 4 * K1_SLOT;
constexpr int K1_LDS = K1_SCR + 256;
constexpr int K1_QS = 232;

__global__ __launch_bounds__(512, 2) void k1_qkv_attn(const u16* __restrict__ x, const u16* __restrict__ wqkv,
                                                      u16* __restrict__ o, const u16* __restrict__ wo,
                                                      const u16* __restrict__ w1, const u16* __restrict__ w2) {
  extern __shared__ __attribute__((aligned(16))) char smem[];
  const int tid = threadIdx.x, lane = tid & 63;
  const int wid8 = __builtin_amdgcn_readfirstlane(tid >> 6);
  const int wid = wid8 & 3;
  const int b = blockIdx.x, xcd = b & 7, bi = b >> 3;
  const int hp = 2 * xcd + (bi & 1), pnl = bi >> 1;
  const int par = hp & 1;
  const int i16 = lane & 15, g = lane >> 4, q4 = i16 >> 2, p4 = i16 & 3;
  const unsigned lds0 = (unsigned)(uintptr_t)smem;
  const int T0 = pnl * 128 + wid * 32;


  if (wid8 >= 4) {
  unsigned wo_[5], xo_[5];
  {
    const int segbase = (72 * hp) & ~15;
#pragma unroll
    for (int j = 0; j < 5; ++j) {
      const int n = j < 4 ? wid + 4 * j : 16 + (wid & 1);
      const int idx = n * 64 + lane;
      const int prow = idx / 18; int ch = idx - prow * 18; ch = ch > 14 ? 14 : ch;
      const int q = (prow >> 2) & 3;
      const int row = (prow & ~15) | ((q == 1 ? 2 : (q == 2 ? 1 : q)) << 2) | (prow & 3);
      const int seg = ch / 5, cc = ch - seg * 5;
      wo_[j] = (unsigned)(row * 3456 + seg * 1152 + segbase + cc * 16);
      const int xrow = idx >> 3, xch = idx & 7;
      int tok = pnl * 128 - 16 + xrow; tok = tok < 0 ? 0 : tok;
      xo_[j] = (unsigned)(tok * 1152 + ((xch ^ (xrow & 7)) * 16));
    }
  }
  auto issue_stage = [&](int st) AINL {
    const unsigned dst = lds0 + (unsigned)((st & 3) * K1_SLOT);
    const char* wb = (const char*)wqkv + (size_t)st * 64 * 3456;
    const char* xbp = (const char*)x + st * 128;
#pragma unroll
    for (int j = 0; j < 4; ++j) {
      glds16(wo_[j], wb, dst + (unsigned)((wid + 4 * j) * 1024));
      glds16(xo_[j], xbp, dst + (unsigned)(K1_XO + (wid + 4 * j) * 1024));
    }
    if (wid < 2) glds16(wo_[4], wb, dst + (unsigned)((16 + wid) * 1024));
    else glds16(xo_[4], xbp, dst + (unsigned)(K1_XO + (16 + wid - 2) * 1024));
  };
    issue_stage(0); issue_stage(1); issue_stage(2); issue_stage(3);
    waitcnt<27>(); barrier();
#pragma unroll 1
    for (int st = 0; st < 9; ++st) {
      if (st < 6) waitcnt<18>(); else if (st == 6) waitcnt<9>(); else waitcnt<0>();
      barrier();
      if (st + 4 < 9) issue_stage(st + 4);
    }
    barrier();
    if (bi < 21) {
      int L = bi * 256 + (tid & 255); L = L > 5183 ? 5183 : L;
      const int row = L / 9, l = L - row * 9;
      const unsigned scr = lds0 + (unsigned)K1_SCR;
      gtouch((unsigned)(row * 9216 + 1152 * xcd + l * 128), w1, scr);
      gtouch((unsigned)(663552 * xcd + L * 128), w2, scr);
      gtouch((unsigned)(L * 128), wo, scr);
    }
    return;
  }


  f32x4 acc[7][3];
#pragma unroll
  for (int ft = 0; ft < 7; ++ft)
#pragma unroll
    for (int ct = 0; ct < 3; ++ct) acc[ft][ct] = f32x4{0.f, 0.f, 0.f, 0.f};
  int aoff[7];
#pragma unroll
  for (int ft = 0; ft < 7; ++ft) {
    int G = 4 * ft + p4; G = G > 26 ? 26 : G;
    const int seg = G / 9, wi = G - seg * 9;
    aoff[ft] = (16 * (g >> 1) + 4 * (g & 1) + q4) * K1_ROW + seg * 80 + par * 8 + wi * 8;
  }
  int boff[3];
#pragma unroll
  for (int ct = 0; ct < 3; ++ct) {
    const int row = wid * 32 + ct * 16 + i16;
    boff[ct] = K1_XO + row * 128 + ((g ^ (row & 7)) * 16);
  }
  f32x4 asum[3], asq[3];
#pragma unroll
  for (int ct = 0; ct < 3; ++ct) { asum[ct] = f32x4{0.f, 0.f, 0.f, 0.f}; asq[ct] = f32x4{0.f, 0.f, 0.f, 0.f}; }
  const s16x8 ones8 = {0x3f80, 0x3f80, 0x3f80, 0x3f80, 0x3f80, 0x3f80, 0x3f80, 0x3f80};
  s16x8 xbA[3], xbB[3];
  s16x4 afA[14], afB[14];
  auto load_job = [&](auto J_, auto KS_, int st, s16x8 (&xb)[3], s16x4 (&af)[14]) AINL {
    constexpr int j = decltype(J_)::value, ks = decltype(KS_)::value;
    lds_cptr sl = (lds_cptr)smem + (st & 3) * K1_SLOT;
    if constexpr (j < 3) xb[j] = *(__attribute__((address_space(3))) const s16x8*)(sl + (boff[j] ^ (ks * 64)));
    else af[j - 3] = vtr(sl + aoff[(j - 3) >> 1] + ks * 32 * K1_ROW + ((j - 3) & 1) * 8 * K1_ROW);
  };
  auto half = [&](auto KS_, int st, s16x8 (&xc)[3], s16x4 (&ac)[14], s16x8 (&xn)[3], s16x4 (&an)[14]) AINL {
    constexpr int ks = decltype(KS_)::value;
    if constexpr (ks == 1) {

      waitcnt<63, 0>();
      barrier();

    }
    s16x8 xh;
    static_for<0, 27>([&](auto M_) AINL {
      constexpr int r = M_;
      if constexpr (r < 3) asq[r] = MFMA16(xc[r], xc[r], asq[r]);
      else if constexpr (r < 6) asum[r - 3] = MFMA16(ones8, xc[r - 3], asum[r - 3]);
      else {
        constexpr int ft = (r - 6) / 3, ct = (r - 6) % 3;
        acc[ft][ct] = MFMA16(cat8(ac[2 * ft], ac[2 * ft + 1]), ct == 0 ? xh : xc[ct], acc[ft][ct]);
      }
      if constexpr (r == 4) { xh = xc[0]; if (i16 == 0) xh = ones8; }
      {
        constexpr int u = r;
        if constexpr (u < 17) load_job(std::integral_constant<int, u>{}, std::integral_constant<int, 1 - ks>{}, st + ks, xn, an);
      }
      __builtin_amdgcn_sched_barrier(0);
    });
  };
  using KS0 = std::integral_constant<int, 0>; using KS1 = std::integral_constant<int, 1>;
  barrier();

  static_for<0, 17>([&](auto J_) AINL { load_job(J_, KS0{}, 0, xbA, afA); });
#pragma unroll 1
  for (int st = 0; st < 9; ++st) {
    half(KS0{}, st, xbA, afA, xbB, afB);
    half(KS1{}, st, xbB, afB, xbA, afA);
  }
  float ssum[3], ssq[3];
#pragma unroll
  for (int ct = 0; ct < 3; ++ct) {
    ssum[ct] = asum[ct][0];
    float dsel = (i16 & 2) ? ((i16 & 1) ? asq[ct][3] : asq[ct][2]) : ((i16 & 1) ? asq[ct][1] : asq[ct][0]);
    ssq[ct] = __shfl(dsel, (i16 >> 2) * 16 + i16);
  }

  WAIT_BAR(0);
  lds_ptr qs = (lds_ptr)smem + wid * (48 * K1_QS);
#pragma unroll
  for (int ct = 0; ct < 3; ++ct) {
    const float mean = ssum[ct] * (1.0f / 576.0f);
    float var = ssq[ct] * (1.0f / 576.0f) - mean * mean;
    var = var < 0.f ? 0.f : var;
    const float rs = 1.0f / sqrtf(var + EPS);
    const float sm = rs * mean;
#pragma unroll
    for (int ft = 0; ft < 7; ++ft) {
      const int G = 4 * ft + g;
      float v[4];
#pragma unroll
      for (int r = 0; r < 4; ++r) {
        const float c = __shfl(acc[ft][0][r], lane & 48);
        v[r] = rs * acc[ft][ct][r] - sm * c;
      }
      if (G < 27) {
        u32x2 pk = {pack_bf16(v[0], v[1]), pack_bf16(v[2], v[3])};
        *(__attribute__((address_space(3))) u32x2*)(qs + (ct * 16 + i16) * K1_QS + G * 8) = pk;
      }
    }
  }
  asm volatile("s_waitcnt lgkmcnt(0)" ::: "memory");

  {
    const int tk = lane & 31, hd = lane >> 5;
    lds_cptr qrow = (lds_cptr)qs + (16 + tk) * K1_QS + hd * 36;
    unsigned qv[9];
#pragma unroll
    for (int d = 0; d < 9; ++d) qv[d] = *(__attribute__((address_space(3))) const unsigned*)(qrow + d * 4);
    float lg[8];
#pragma unroll
    for (int w = 0; w < 8; ++w) {
      lds_cptr krow = qrow - 2 * w * K1_QS + 72;
      float a = 0.f;
#pragma unroll
      for (int d = 0; d < 9; ++d) a = dot2bf(qv[d], *(__attribute__((address_space(3))) const unsigned*)(krow + d * 4), a);
      lg[w] = (T0 + tk - 2 * w >= 0) ? a * 0.23570226039551584f : -1e9f;
    }
    float mx = lg[0];
#pragma unroll
    for (int w = 1; w < 8; ++w) mx = fmaxf(mx, lg[w]);
    float se = 0.f;
#pragma unroll
    for (int w = 0; w < 8; ++w) { lg[w] = __builtin_amdgcn_exp2f((lg[w] - mx) * 1.4426950408889634f); se += lg[w]; }
    const float inv = 1.0f / se;
    float ov[18];
#pragma unroll
    for (int d = 0; d < 18; ++d) ov[d] = 0.f;
#pragma unroll
    for (int w = 0; w < 8; ++w) {
      lds_cptr vrow = qrow - 2 * w * K1_QS + 144;
      const float pw = lg[w] * inv;
#pragma unroll
      for (int d = 0; d < 9; ++d) {
        const unsigned u = *(__attribute__((address_space(3))) const unsigned*)(vrow + d * 4);
        ov[2 * d] = __builtin_fmaf(pw, bf_lo(u), ov[2 * d]);
        ov[2 * d + 1] = __builtin_fmaf(pw, bf_hi(u), ov[2 * d + 1]);
      }
    }
    {
      const int tok = T0 + tk, c0 = hp * 36 + hd * 18;
      char* ob_ = (char*)o + (size_t)((tok >> 4) * 18 * 64 + (tok & 15)) * 16;
#pragma unroll
      for (int d = 0; d < 9; ++d) {
        const int col = c0 + 2 * d;
        *(unsigned*)(ob_ + (col >> 3) * 256 + (col & 7) * 2) = pack_bf16(ov[2 * d], ov[2 * d + 1]);
      }
    }
  }

}

constexpr int K2_LDS = 56 * 1024;

__global__ __launch_bounds__(256, 1) void k2_proj(const u16* __restrict__ o, const u16* __restrict__ x,
                                                  const u16* __restrict__ wo, const u16* __restrict__ w1,
                                                  u16* __restrict__ h, float* __restrict__ stats, float* __restrict__ c1) {
  extern __shared__ __attribute__((aligned(16))) char smem[];
  const int tid = threadIdx.x, lane = tid & 63;
  const int b = blockIdx.x;
  if (b >= 192) {
    const int cb = b - 192;
    const int cg = tid % 9, rg = tid / 9;
    float* red = (float*)smem;
    if (rg < 24) {
      u32x4 v[24];
#pragma unroll
      for (int i = 0; i < 24; ++i) v[i] = *(const u32x4*)(w1 + (size_t)(rg * 24 + i) * F_ + cb * 72 + cg * 8);
      float s[8];
#pragma unroll
      for (int e = 0; e < 8; ++e) s[e] = 0.f;
#pragma unroll
      for (int i = 0; i < 24; ++i)
#pragma unroll
        for (int e = 0; e < 4; ++e) { s[2 * e] += bf_lo(v[i][e]); s[2 * e + 1] += bf_hi(v[i][e]); }
#pragma unroll
      for (int e = 0; e < 8; ++e) red[rg * 72 + cg * 8 + e] = s[e];
    }
    __syncthreads();
    if (tid < 72) {
      float s = 0.f;
      for (int r = 0; r < 24; ++r) s += red[r * 72 + tid];
      c1[cb * 72 + tid] = s;
    }
    return;
  }
  const int wid = __builtin_amdgcn_readfirstlane(tid >> 6);

  const int xcd = b & 7, bi = b >> 3;
  const int pnl = 2 * xcd + (bi >= 12 ? 1 : 0), n = bi >= 12 ? bi - 12 : bi;
  const int i16 = lane & 15, g = lane >> 4, q4 = i16 >> 2, p4 = i16 & 3;
  const unsigned lds0 = (unsigned)(uintptr_t)smem;
  const int T0 = pnl * 128 + wid * 32;

  s16x8 ob[2][18];
#pragma unroll
  for (int ct = 0; ct < 2; ++ct) {
    const u32x4* op_ = (const u32x4*)o + (size_t)((T0 >> 4) + ct) * 18 * 64 + lane;
#pragma unroll
    for (int ks = 0; ks < 18; ++ks) ob[ct][ks] = __builtin_bit_cast(s16x8, op_[ks * 64]);
  }
#pragma unroll
  for (int j = 0; j < 14; ++j) {
    int ni = j * 4 + wid; ni = ni > 53 ? 53 : ni;
    const int idx = ni * 64 + lane;
    const int row = idx / 6, ch = idx - row * 6;
    glds16((unsigned)(row * 1152 + n * 96 + ch * 16), wo, lds0 + (unsigned)(ni * 1024));
  }
  u32x2 xr[2][3];
#pragma unroll
  for (int ct = 0; ct < 2; ++ct)
#pragma unroll
    for (int ft = 0; ft < 3; ++ft)
      xr[ct][ft] = *(const u32x2*)(x + (size_t)(T0 + ct * 16 + i16) * D_ + n * 48 + ft * 16 + 4 * g);

  f32x4 acc[3][2];
#pragma unroll
  for (int ft = 0; ft < 3; ++ft)
#pragma unroll
    for (int ct = 0; ct < 2; ++ct) acc[ft][ct] = f32x4{0.f, 0.f, 0.f, 0.f};
  lds_cptr abase = (lds_cptr)smem + (8 * g + q4) * 96 + p4 * 8;

  WAIT_BAR(0);

#pragma unroll
  for (int ks = 0; ks < 18; ++ks)
#pragma unroll
    for (int ft = 0; ft < 3; ++ft) {
      s16x8 a = cat8(vtr(abase + ks * 32 * 96 + ft * 32), vtr(abase + ks * 32 * 96 + ft * 32 + 4 * 96));
#pragma unroll
      for (int ct = 0; ct < 2; ++ct) acc[ft][ct] = MFMA16(a, ob[ct][ks], acc[ft][ct]);
    }
#pragma unroll
  for (int ct = 0; ct < 2; ++ct) {
    const int tok = T0 + ct * 16 + i16;
    float hv[12];
#pragma unroll
    for (int ft = 0; ft < 3; ++ft) {
      const unsigned p0 = pack_bf16(bf_lo(xr[ct][ft][0]) + acc[ft][ct][0], bf_hi(xr[ct][ft][0]) + acc[ft][ct][1]);
      const unsigned p1 = pack_bf16(bf_lo(xr[ct][ft][1]) + acc[ft][ct][2], bf_hi(xr[ct][ft][1]) + acc[ft][ct][3]);
      u32x2 pk = {p0, p1};
      {
        const int col = n * 48 + ft * 16 + 4 * g;
        const int ks = col >> 4, hh2 = (col >> 3) & 1, j4 = col & 4;
        *(u32x2*)(h + ((size_t)(((tok >> 5) * 36 + ks) * 64 + hh2 * 32 + (tok & 31)) * 8 + j4)) = pk;
      }
      hv[ft * 4 + 0] = bf_lo(p0); hv[ft * 4 + 1] = bf_hi(p0); hv[ft * 4 + 2] = bf_lo(p1); hv[ft * 4 + 3] = bf_hi(p1);
    }
    float s = 0.f;
#pragma unroll
    for (int e = 0; e < 12; ++e) s += hv[e];
    s += __shfl_xor(s, 16); s += __shfl_xor(s, 32);
    const float mean = s * (1.0f / 48.0f);
    float m2 = 0.f;
#pragma unroll
    for (int e = 0; e < 12; ++e) { const float d = hv[e] - mean; m2 = __builtin_fmaf(d, d, m2); }
    m2 += __shfl_xor(m2, 16); m2 += __shfl_xor(m2, 32);
    if (g == 0) { stats[(size_t)tok * 24 + n * 2] = s; stats[(size_t)tok * 24 + n * 2 + 1] = m2; }
  }

}

constexpr int HS = 18432;
constexpr int K3_XR = 4 * HS;
constexpr int K3_TAB = K3_XR + 73728;
constexpr int K3_SCR = K3_TAB + 288 * 8;
constexpr int K3_LDS = K3_SCR + 256;

__global__ __launch_bounds__(512, 2) void k3_mlp(const u32x4* __restrict__ hT, const float* __restrict__ stats,
                                                 const float* __restrict__ c1, const u16* __restrict__ w1,
                                                 const u16* __restrict__ b1, const u16* __restrict__ w2,
                                                 u32x4* __restrict__ part) {
  extern __shared__ __attribute__((aligned(16))) char smem[];
  const int tid = threadIdx.x, lane = tid & 63;
  const int wid = __builtin_amdgcn_readfirstlane(tid >> 6);
  const int b = blockIdx.x, xcd = b & 7, bi = b >> 3;
  const int c = 2 * xcd + (bi & 1), pnl = bi >> 1;
  const unsigned lds0 = (unsigned)(uintptr_t)smem;


  if (wid >= 4) {
    const int lw = wid - 4;
    unsigned o1[5], o2[5];
#pragma unroll
    for (int j = 0; j < 5; ++j) {
      const int n = j < 4 ? lw + 4 * j : 16 + (lw & 1);
      const int row = n * 16 + (lane >> 2), ch = lane & 3;
      o1[j] = (unsigned)(row * (F_ * 2) + ch * 16);
      o2[j] = (unsigned)(row * (D_ * 2) + ch * 16);
    }
    const char* w1c = (const char*)w1 + (size_t)(288 * c) * 2;
    const char* w2c = (const char*)w2 + (size_t)(288 * c) * D_ * 2;
    auto issue_step = [&](int s) AINL {
      const bool is2 = s >= 18;
      const char* base = is2 ? w2c + (size_t)(64 * (s - 18)) : w1c + ((size_t)(288 * (s & 1)) * F_ + 32 * (s >> 1)) * 2;
      const unsigned dst = lds0 + (unsigned)((s & 3) * HS);
#pragma unroll
      for (int j = 0; j < 4; ++j) glds16(is2 ? o2[j] : o1[j], base, dst + (unsigned)((lw + 4 * j) * 1024));
      if ((lw >> 1) == (s & 1)) glds16(is2 ? o2[4] : o1[4], base, dst + (unsigned)((16 + (lw & 1)) * 1024));
    };
    issue_step(0); issue_step(1); issue_step(2);
    {
      float* tc = (float*)(smem + K3_TAB);
      const int t = tid - 256;
      tc[t] = c1[288 * c + t]; tc[288 + t] = bf_lo(b1[288 * c + t]);
      if (t < 32) { tc[256 + t] = c1[288 * c + 256 + t]; tc[288 + 256 + t] = bf_lo(b1[288 * c + 256 + t]); }
    }
    waitcnt<4, 0>(); barrier();
#pragma unroll 1
    for (int s = 0; s < 36; ++s) {
      if (s + 3 < 36) issue_step(s + 3);
      if (s >= 20) {
        const int d = s - 20;
        lds_cptr sp = (lds_cptr)smem + K3_XR + lw * 18432 + (d & 1) * 2048 + lane * 16;
        const u32x4 q0 = *(__attribute__((address_space(3))) const u32x4*)sp;
        const u32x4 q1 = *(__attribute__((address_space(3))) const u32x4*)(sp + 1024);
        const unsigned dq = (unsigned)(((((c * 16 + pnl) * 4 + lw) * 36) + 2 * d) * 64 + lane) * 16u;
        st_part(part, dq, q0); st_part(part, dq + 1024, q1);
        if (s + 3 < 36) waitcnt<6>(); else waitcnt<2>();
      } else {
        if (s + 3 < 36) waitcnt<4>(); else waitcnt<0>();
      }
      barrier();
    }
    {
      lds_cptr sp = (lds_cptr)smem + K3_XR + lw * 18432 + lane * 16;
      const u32x4 q0 = *(__attribute__((address_space(3))) const u32x4*)sp;
      const u32x4 q1 = *(__attribute__((address_space(3))) const u32x4*)(sp + 1024);
      const unsigned dq = (unsigned)(((((c * 16 + pnl) * 4 + lw) * 36) + 2 * 16) * 64 + lane) * 16u;
      st_part(part, dq, q0); st_part(part, dq + 1024, q1);
    }
    return;
  }

  const int t32 = lane & 31, hh = lane >> 5;
  const int tok = pnl * 128 + wid * 32 + t32;
  const int i16 = lane & 15, q4 = i16 >> 2, p4 = i16 & 3, blk = (lane >> 4) & 1;
  s16x8 hb[36];
  {
    const u32x4* hp = hT + (size_t)((pnl * 4 + wid) * 36) * 64 + lane;
#pragma unroll
    for (int ks = 0; ks < 36; ++ks) hb[ks] = __builtin_bit_cast(s16x8, hp[ks * 64]);
  }
  float sc, scm;
  {
    f32x4 stv[6];
#pragma unroll
    for (int i = 0; i < 6; ++i) stv[i] = *(const f32x4*)(stats + (size_t)tok * 24 + i * 4);
    float sum = 0.f;
#pragma unroll
    for (int i = 0; i < 6; ++i) sum += stv[i][0] + stv[i][2];
    const float mean = sum * (1.0f / 576.0f);
    float m2 = 0.f;
#pragma unroll
    for (int i = 0; i < 6; ++i) {
      const float d0 = stv[i][0] * (1.0f / 48.0f) - mean, d1 = stv[i][2] * (1.0f / 48.0f) - mean;
      m2 += stv[i][1] + stv[i][3] + 48.0f * (d0 * d0 + d1 * d1);
    }
    const float var = m2 * (1.0f / 576.0f);
    const float r1 = __builtin_amdgcn_rsqf(var + EPS);
    const float var2 = var * r1 * r1;
    const float r2 = __builtin_amdgcn_rsqf(var2 + EPS);
    sc = r1 * r2; scm = sc * mean;
  }
  const int lch = (16 * blk + 4 * p4) * 2;
  lds_cptr lb1 = (lds_cptr)smem + (8 * hh + q4) * 64 + lch;
  lds_cptr lb2 = (lds_cptr)smem + (4 * hh + q4) * 64 + lch;
  lds_ptr xw = (lds_ptr)smem + K3_XR + wid * 18432 + lane * 16;

  constexpr int LA = 4;
  s16x8 fr[LA];
  f32x16 accA, accB, zero16;
#pragma unroll
  for (int r = 0; r < 16; ++r) { accA[r] = 0.f; accB[r] = 0.f; zero16[r] = 0.f; }
  using TT = std::true_type; using FF = std::false_type;
  using P0 = std::integral_constant<int, 0>; using P2 = std::integral_constant<int, 2>;


  waitcnt<0, 0>(); barrier();

  static_for<0, LA>([&](auto M) AINL {
    constexpr int m = M;
    lds_cptr a = lb1 + m * 16 * 64;
    fr[m] = cat8(vtr(a), vtr(a + 4 * 64));
  });

  auto pass1 = [&](auto LAST_, f32x16& acc, const f32x16& accp, int it) AINL {
    constexpr bool LAST = decltype(LAST_)::value;
    const int ftp = it == 0 ? 0 : it - 1;
    const int oA = ((2 * it) & 3) * HS, oN = ((2 * it + 2) & 3) * HS;
    unsigned gp[8];
    float v0 = 0.f, v1 = 0.f, u0 = 0.f, u1 = 0.f;
    f32x4 tcc, tbb;
    static_for<0, 36>([&](auto MM) AINL {
      constexpr int m = MM;
      if constexpr (m == 18) barrier();
      acc = MFMA32(fr[m % LA], hb[m], m == 0 ? zero16 : acc);
      constexpr int mn = m + LA;
      lds_cptr a = mn < 36 ? lb1 + oA + (mn / 18) * HS + (mn % 18) * 16 * 64 : (LAST ? lb2 : lb1) + oN + (mn - 36) * 16 * 64;
      constexpr int r2 = (mn >= 36 && LAST) ? 8 * 64 : 4 * 64;
      s16x4 flo, fhi;
      constexpr int ph = (m - 4) & 3, e = ((m - 4) >> 2) * 2;
      const float gc1 = -2.0f * 1.4426950408889634f * 0.7978845608028654f, gc3 = gc1 * 0.044715f;
      if constexpr (m >= 4 && ph == 0) {
        u0 = __builtin_fmaf(-scm, tcc[e & 3], tbb[e & 3]); u1 = __builtin_fmaf(-scm, tcc[(e & 3) + 1], tbb[(e & 3) + 1]);
        flo = vtr(a);
        v0 = __builtin_fmaf(sc, accp[e], u0); v1 = __builtin_fmaf(sc, accp[e + 1], u1);
        fhi = vtr(a + r2);
        u0 = v0 * v0;
      } else if constexpr (m >= 4 && ph == 1) {
        u1 = v1 * v1; u0 = __builtin_fmaf(u0, gc3, gc1);
        flo = vtr(a);
        u1 = __builtin_fmaf(u1, gc3, gc1); u0 = v0 * u0;
        fhi = vtr(a + r2);
        u1 = v1 * u1;
      } else if constexpr (m >= 4 && ph == 2) {
        u0 = __builtin_amdgcn_exp2f(u0); u1 = __builtin_amdgcn_exp2f(u1);
        flo = vtr(a); fhi = vtr(a + r2);
        u0 = 1.0f + u0; u1 = 1.0f + u1;
      } else if constexpr (m >= 4 && ph == 3) {
        u0 = __builtin_amdgcn_rcpf(u0); u1 = __builtin_amdgcn_rcpf(u1);
        flo = vtr(a); fhi = vtr(a + r2);
        gp[e >> 1] = pack_bf16(v0 * u0, v1 * u1);
        asm volatile("" : "+v"(gp[e >> 1]));
      } else { flo = vtr(a); fhi = vtr(a + r2); }
      asm volatile("" : "+v"(v0), "+v"(v1), "+v"(u0), "+v"(u1));
      if constexpr ((m & 7) == 1 && m < 32) {
        lds_cptr tc = (lds_cptr)smem + K3_TAB + (32 * ftp + 4 * hh + m - 1) * 4;
        tcc = *(__attribute__((address_space(3))) const f32x4*)tc;
        tbb = *(__attribute__((address_space(3))) const f32x4*)(tc + 288 * 4);
      }
      if constexpr (m == 20) *(__attribute__((address_space(3))) u32x4*)(xw + (ftp * 2) * 1024) = u32x4{gp[0], gp[1], gp[2], gp[3]};
      fr[m % LA] = cat8(flo, fhi);
      __builtin_amdgcn_sched_barrier(0);
    });
    *(__attribute__((address_space(3))) u32x4*)(xw + (ftp * 2 + 1) * 1024) = u32x4{gp[4], gp[5], gp[6], gp[7]};
    barrier();
  };
#pragma unroll 1
  for (int i2 = 0; i2 < 4; ++i2) {
    pass1(FF{}, accA, accB, 2 * i2);
    pass1(FF{}, accB, accA, 2 * i2 + 1);
  }
  pass1(TT{}, accA, accB, 8);

  {
    float gv[16];
#pragma unroll
    for (int rg = 0; rg < 4; ++rg) {
      lds_cptr tc = (lds_cptr)smem + K3_TAB + (32 * 8 + 4 * hh + 8 * rg) * 4;
      const f32x4 cc = *(__attribute__((address_space(3))) const f32x4*)tc;
      const f32x4 bb = *(__attribute__((address_space(3))) const f32x4*)(tc + 288 * 4);
#pragma unroll
      for (int e = 0; e < 4; ++e) gv[rg * 4 + e] = gelu_tanh(__builtin_fmaf(sc, accA[rg * 4 + e], __builtin_fmaf(-scm, cc[e], bb[e])));
    }
#pragma unroll
    for (int s = 0; s < 2; ++s) {
      u32x4 pk = {pack_bf16(gv[8 * s], gv[8 * s + 1]), pack_bf16(gv[8 * s + 2], gv[8 * s + 3]),
                  pack_bf16(gv[8 * s + 4], gv[8 * s + 5]), pack_bf16(gv[8 * s + 6], gv[8 * s + 7])};
      *(__attribute__((address_space(3))) u32x4*)(xw + (16 + s) * 1024) = pk;
    }
  }
  s16x8 xbf[18];
#pragma unroll
  for (int k = 0; k < 18; ++k) xbf[k] = *(__attribute__((address_space(3))) const s16x8*)(xw + k * 1024);


  u32x4* dst = part + (size_t)(((c * 16 + pnl) * 4 + wid) * 36) * 64 + lane;
  auto pass2 = [&](auto LAST_, auto PH_, f32x16& acc, const f32x16& accp, int d) AINL {
    constexpr bool LAST = decltype(LAST_)::value;
    constexpr int PH = decltype(PH_)::value;
    const int oA = ((18 + d) & 3) * HS, oN = ((19 + d) & 3) * HS;
    lds_ptr sw = xw + ((d + 1) & 1) * 2048;
    u32x4 pk0, pk1;
    static_for<0, 18>([&](auto MM) AINL {
      constexpr int m = MM;
      acc = MFMA32(fr[(m + PH) % LA], xbf[m], m == 0 ? zero16 : acc);
      constexpr int mn = m + LA;
      if constexpr (mn < 18) { lds_cptr a = lb2 + oA + mn * 16 * 64; fr[(m + PH) % LA] = cat8(vtr(a), vtr(a + 8 * 64)); }
      else if constexpr (!LAST) { lds_cptr a = lb2 + oN + (mn - 18) * 16 * 64; fr[(m + PH) % LA] = cat8(vtr(a), vtr(a + 8 * 64)); }
      if constexpr (m >= 2 && m < 6) { pk0[m - 2] = pack_bf16(accp[2 * (m - 2)], accp[2 * (m - 2) + 1]); pk1[m - 2] = pack_bf16(accp[8 + 2 * (m - 2)], accp[8 + 2 * (m - 2) + 1]); }
      if constexpr (m == 6) *(__attribute__((address_space(3))) u32x4*)sw = pk0;
      if constexpr (m == 7) *(__attribute__((address_space(3))) u32x4*)(sw + 1024) = pk1;
      __builtin_amdgcn_sched_barrier(0);
    });
    if constexpr (!LAST) barrier();
  };
#pragma unroll 1
  for (int d2 = 0; d2 < 8; ++d2) {
    pass2(FF{}, P0{}, accA, accB, 2 * d2);
    pass2(FF{}, P2{}, accB, accA, 2 * d2 + 1);
  }
  pass2(FF{}, P0{}, accA, accB, 16);
  pass2(TT{}, P2{}, accB, accA, 17);
  barrier();
#pragma unroll
  for (int h2 = 0; h2 < 2; ++h2) {
    u32x4 pk = {pack_bf16(accB[8 * h2], accB[8 * h2 + 1]), pack_bf16(accB[8 * h2 + 2], accB[8 * h2 + 3]),
                pack_bf16(accB[8 * h2 + 4], accB[8 * h2 + 5]), pack_bf16(accB[8 * h2 + 6], accB[8 * h2 + 7])};
    st_part(part, (unsigned)((((c * 16 + pnl) * 4 + wid) * 36 + 17 * 2 + h2) * 64 + lane) * 16u, pk);
  }

}

__global__ __launch_bounds__(256) void k4_reduce(const u32x4* __restrict__ part, const u16* __restrict__ h,
                                                 const u16* __restrict__ b2, u16* __restrict__ out) {
  const int gid = blockIdx.x * 256 + threadIdx.x;
  const int lane = gid & 63, rest = gid >> 6;
  const int h2 = rest & 1, dtl = (rest >> 1) & 1, r1 = rest >> 2;
  const int dp = r1 % 9, r3 = r1 / 9;
  const int t32 = lane & 31, hh = lane >> 5;
  const int tok = r3 * 32 + t32;
  const int d0 = 64 * dp + 32 * dtl + 16 * h2 + 4 * hh;
  u32x4 v[16];
#pragma unroll
  for (int c = 0; c < 16; ++c) v[c] = part[(size_t)c * 147456 + gid];
  const u16* hp = h + ((size_t)((r3 * 36 + (d0 >> 4)) * 64 + t32) * 8 + 4 * hh);
  const u32x2 ha = *(const u32x2*)hp, hb = *(const u32x2*)(hp + 256);
  const u32x2 ba = *(const u32x2*)(b2 + d0), bb = *(const u32x2*)(b2 + d0 + 8);
  float s[8];
  s[0] = bf_lo(ha[0]) + bf_lo(ba[0]); s[1] = bf_hi(ha[0]) + bf_hi(ba[0]);
  s[2] = bf_lo(ha[1]) + bf_lo(ba[1]); s[3] = bf_hi(ha[1]) + bf_hi(ba[1]);
  s[4] = bf_lo(hb[0]) + bf_lo(bb[0]); s[5] = bf_hi(hb[0]) + bf_hi(bb[0]);
  s[6] = bf_lo(hb[1]) + bf_lo(bb[1]); s[7] = bf_hi(hb[1]) + bf_hi(bb[1]);
  float a[8];
#pragma unroll
  for (int e = 0; e < 8; ++e) a[e] = 0.f;
#pragma unroll
  for (int c = 0; c < 16; ++c)
#pragma unroll
    for (int e = 0; e < 4; ++e) { a[2 * e] += bf_lo(v[c][e]); a[2 * e + 1] += bf_hi(v[c][e]); }
  u32x2 oa = {pack_bf16(s[0] + a[0], s[1] + a[1]), pack_bf16(s[2] + a[2], s[3] + a[3])};
  u32x2 ob = {pack_bf16(s[4] + a[4], s[5] + a[5]), pack_bf16(s[6] + a[6], s[7] + a[7])};
  *(u32x2*)(out + (size_t)tok * D_ + d0) = oa;
  *(u32x2*)(out + (size_t)tok * D_ + d0 + 8) = ob;
}

extern "C" void kernel_launch(void* const* d_in, const int* in_sizes, int n_in, void* d_out, int out_size,
                              void* d_ws, size_t ws_size, hipStream_t stream) {
  const u16* x = (const u16*)d_in[0];
  const u16* wqkv = (const u16*)d_in[1];
  const u16* wo = (const u16*)d_in[2];
  const u16* w1 = (const u16*)d_in[4];
  const u16* b1 = (const u16*)d_in[5];
  const u16* w2 = (const u16*)d_in[6];
  const u16* b2 = (const u16*)d_in[7];
  u16* out = (u16*)d_out;
  char* ws = (char*)d_ws;
  u16* o = (u16*)ws;
  u16* h = (u16*)(ws + 2359296);
  float* stats = (float*)(ws + 2 * 2359296);
  float* c1 = (float*)(ws + 2 * 2359296 + 196608);
  u32x4* part = (u32x4*)(ws + 2 * 2359296 + 196608 + 18432 + 2048);

  hipFuncSetAttribute((const void*)k1_qkv_attn, hipFuncAttributeMaxDynamicSharedMemorySize, K1_LDS);
  hipFuncSetAttribute((const void*)k2_proj, hipFuncAttributeMaxDynamicSharedMemorySize, K2_LDS);
  hipFuncSetAttribute((const void*)k3_mlp, hipFuncAttributeMaxDynamicSharedMemorySize, K3_LDS);

  k1_qkv_attn<<<256, 512, K1_LDS, stream>>>(x, wqkv, o, wo, w1, w2);
  k2_proj<<<256, 256, K2_LDS, stream>>>(o, x, wo, w1, h, stats, c1);
  k3_mlp<<<256, 512, K3_LDS, stream>>>((const u32x4*)h, stats, c1, w1, b1, w2, part);
  k4_reduce<<<576, 256, 0, stream>>>(part, h, b2, out);
}
```
